```python
import torch

def _softmax(x, axis=-1):
    return torch.softmax(x, dim=axis)
def _gelu(x):
    return torch.nn.functional.gelu(x, approximate="tanh")
def _silu(x):
    return torch.nn.functional.silu(x)
def _sigmoid(x):
    return torch.sigmoid(x)
def _layernorm(x, axis=-1, eps=1e-5):
    m = x.mean(dim=axis, keepdim=True)
    v = x.var(dim=axis, keepdim=True, unbiased=False)
    return (x - m) / torch.sqrt(v + eps)
def _scatter_add(tgt, idx, src, axis=0):
    return tgt.clone().index_add_(axis, idx, src)
def _segment_sum(src, seg, n):
    out = torch.zeros((n,) + src.shape[1:], dtype=src.dtype)
    return out.index_add_(0, seg, src)
def _pad(x, pw):
    flat = tuple(v for pair in reversed(pw) for v in pair)
    return torch.nn.functional.pad(x, flat)

kernel_name = 'concat_heads__gqa_block_cached__mha_block_cached__decode'

def setup_inputs():

    _w = (lambda a: (lambda t: t.to(torch.bfloat16) if t.is_floating_point() else t)(torch.as_tensor(a)))
    def _A_setup():
        B, Sq, D = 2, 1, 576
        Hq, Hkv, Sk, Dh = 32, 16, 8192, 128
        assert Hq % Hkv == 0, (Hq, Hkv)
        return {
            "x": _w((lambda *s: torch.randn(*s, dtype=torch.bfloat16) * 0.8)(B, Sq, D)),
            "k_cache": _w((lambda *s: torch.randn(*s, dtype=torch.bfloat16) * 0.8)(B, Hkv, Sk, Dh)),
            "v_cache": _w((lambda *s: torch.randn(*s, dtype=torch.bfloat16) * 0.05)(B, Hkv, Sk, Dh)),
            "wq": _w((lambda *s: torch.randn(*s, dtype=torch.bfloat16) * 0.8)(D, Hq * Dh) * (D ** -0.5)),
            "wo": _w((lambda *s: torch.randn(*s, dtype=torch.bfloat16) * 0.05)(Hq * Dh, D) * ((Hq * Dh) ** -0.5)),
        }
    _A_in = _A_setup()

    def _B_setup():
        B, Sq, D = 2, 1, 576
        H, Sk, Dh = 32, 8192, 128
        return {
            "x": _w((lambda *s: torch.randn(*s, dtype=torch.bfloat16) * 0.8)(B, Sq, D)),
            "k_cache": _w((lambda *s: torch.randn(*s, dtype=torch.bfloat16) * 0.8)(B, H, Sk, Dh)),
            "v_cache": _w((lambda *s: torch.randn(*s, dtype=torch.bfloat16) * 0.05)(B, H, Sk, Dh)),
            "wq": _w((lambda *s: torch.randn(*s, dtype=torch.bfloat16) * 0.8)(D, H * Dh) * (D ** -0.5)),
            "wo": _w((lambda *s: torch.randn(*s, dtype=torch.bfloat16) * 0.05)(H * Dh, D) * ((H * Dh) ** -0.5)),
        }
    _B_in = _B_setup()

    return {
        "A_x": _A_in["x"],
        "A_k_cache": _A_in["k_cache"],
        "A_v_cache": _A_in["v_cache"],
        "A_wq": _A_in["wq"],
        "A_wo": _A_in["wo"],
        "B_x": _B_in["x"],
        "B_k_cache": _B_in["k_cache"],
        "B_v_cache": _B_in["v_cache"],
        "B_wq": _B_in["wq"],
        "B_wo": _B_in["wo"],
    }

def reference(A_x, A_k_cache, A_v_cache, A_wq, A_wo, B_x, B_k_cache, B_v_cache, B_wq, B_wo):
    def _A_ref(x, k_cache, v_cache, wq, wo):
        B, Sq, D = x.shape
        Hq, Dh = 32, 128
        Hkv = k_cache.shape[1]
        rep = Hq // Hkv
        q = (lambda x, *d: x.permute(*d))((x @ wq).reshape(B, Sq, Hq, Dh), 0, 2, 1, 3)
        k = (lambda x, n, axis: torch.repeat_interleave(x, n, dim=axis))(k_cache, rep, axis=1)
        v = (lambda x, n, axis: torch.repeat_interleave(x, n, dim=axis))(v_cache, rep, axis=1)
        scores = torch.einsum("bhqd,bhkd->bhqk", q, k) * (Dh ** -0.5)
        p = _softmax(scores, axis=-1)
        o = torch.einsum("bhqk,bhkd->bhqd", p, v)
        return (lambda x, *d: x.permute(*d))(o, 0, 2, 1, 3).reshape(B, Sq, Hq * Dh) @ wo

    def _B_ref(x, k_cache, v_cache, wq, wo):
        B, Sq, D = x.shape
        H, Dh = 32, 128
        q = (lambda x, *d: x.permute(*d))((x @ wq).reshape(B, Sq, H, Dh), 0, 2, 1, 3)
        scores = torch.einsum("bhqd,bhkd->bhqk", q, k_cache) * (Dh ** -0.5)
        p = _softmax(scores, axis=-1)
        o = torch.einsum("bhqk,bhkd->bhqd", p, v_cache)
        return (lambda x, *d: x.permute(*d))(o, 0, 2, 1, 3).reshape(B, Sq, H * Dh) @ wo

    a = _A_ref(A_x, A_k_cache, A_v_cache, A_wq, A_wo)
    b = _B_ref(B_x, B_k_cache, B_v_cache, B_wq, B_wo)
    return torch.concatenate([a, b], axis=-1)
```

```cpp
#include <hip/hip_runtime.h>
#include <hip/hip_bf16.h>
#include <stdint.h>

typedef __attribute__((ext_vector_type(4))) unsigned u32x4;
typedef __attribute__((ext_vector_type(2))) unsigned u32x2;
typedef __attribute__((ext_vector_type(4))) float f32x4;
typedef __attribute__((ext_vector_type(2))) float f32x2;
typedef __attribute__((ext_vector_type(2))) __bf16 bf16x2;

#define NWA 6
#define NWB 5
#define KV_NT 1
#define QBAR 1
#define AOFF 1

#define TS(i) do {} while (0)
constexpr int DM = 576, DH = 128, SK = 8192, NQ = 4096;
constexpr int WAVES = 8, NTHR = WAVES * 64;
constexpr int TILES = SK / 16;
constexpr int SMAX = 8;
constexpr int A_PER_X = 2 * NWA, B_PER_X = 4 * NWB, WG_PER_X = A_PER_X + B_PER_X;
constexpr int GRID1 = 8 * WG_PER_X;
constexpr float C2 = 0.088388347648318440f * 1.4426950408889634f;

constexpr size_t WS_ML = 0, WS_Y = 65536;
constexpr int LDS_REC = 4096, LDS_ML = LDS_REC + 2048, LDS_CNT = LDS_ML + 32, LDS_Y = LDS_CNT + 4, LDS_FLOATS = LDS_Y + 2 * 8 * 2 * DM;

struct Args {
  const unsigned short* x[2];
  const unsigned short* k[2];
  const unsigned short* v[2];
  const unsigned short* wq[2];
  const unsigned short* wo[2];
  float* ws;
};

__device__ __forceinline__ float dot2(unsigned a, unsigned b, float acc) {
  return __builtin_amdgcn_fdot2_f32_bf16(__builtin_bit_cast(bf16x2, a), __builtin_bit_cast(bf16x2, b), acc, false);
}
__device__ __forceinline__ unsigned cvtpk(float lo, float hi) {
  unsigned r; asm("v_cvt_pk_bf16_f32 %0, %1, %2" : "=v"(r) : "v"(lo), "v"(hi)); return r;
}
template <int CTRL> __device__ __forceinline__ float dpp(float x) {
  return __builtin_bit_cast(float, __builtin_amdgcn_mov_dpp(__builtin_bit_cast(int, x), CTRL, 0xf, 0xf, true));
}
constexpr int XOR1 = 0xB1, XOR2 = 0x4E, XOR7 = 0x141, XOR8 = 0x128;
__device__ __forceinline__ float row_sum16(float x) {
  x += dpp<XOR8>(x); x += dpp<XOR7>(x); x += dpp<XOR2>(x); x += dpp<XOR1>(x); return x;
}
__device__ __forceinline__ float xrow_max(float x) {
  auto s = __builtin_amdgcn_permlane16_swap(__float_as_uint(x), __float_as_uint(x), false, false);
  x = fmaxf(__uint_as_float(s[0]), __uint_as_float(s[1]));
  auto t = __builtin_amdgcn_permlane32_swap(__float_as_uint(x), __float_as_uint(x), false, false);
  return fmaxf(__uint_as_float(t[0]), __uint_as_float(t[1]));
}
__device__ __forceinline__ float xrow_sum(float x) {
  auto s = __builtin_amdgcn_permlane16_swap(__float_as_uint(x), __float_as_uint(x), false, false);
  x = __uint_as_float(s[0]) + __uint_as_float(s[1]);
  auto t = __builtin_amdgcn_permlane32_swap(__float_as_uint(x), __float_as_uint(x), false, false);
  return __uint_as_float(t[0]) + __uint_as_float(t[1]);
}
__device__ __forceinline__ float bflo(unsigned u) { return __uint_as_float(u << 16); }
__device__ __forceinline__ float bfhi(unsigned u) { return __uint_as_float(u & 0xffff0000u); }
typedef const __attribute__((address_space(1))) char* gptr;
typedef const __attribute__((address_space(1))) u32x4* gvec;
__device__ __forceinline__ u32x4 ldkv(gptr base, unsigned off) {
  return __builtin_nontemporal_load((gvec)(base + off));
}
__device__ __forceinline__ u32x4 ld16(gptr base, unsigned off) { return *(gvec)(base + off); }

struct Tile { u32x4 k[4]; u32x4 v[4]; };

__device__ __forceinline__ void load_tile(Tile& t, gptr kp, gptr vp, unsigned loff) {
#pragma unroll
  for (int i = 0; i < 4; ++i) t.k[i] = ldkv(kp + i * 1024, loff);
#pragma unroll
  for (int i = 0; i < 4; ++i) t.v[i] = ldkv(vp + i * 1024, loff);
}

template <int G>
__device__ __forceinline__ void compute_tile(const Tile& t, const u32x4 (&q)[G], float (&m)[G], float (&l)[G], f32x2 (&acc)[G][4]) {
  float p[G][4];
#pragma unroll
  for (int g = 0; g < G; ++g) {
    float s[4];
#pragma unroll
    for (int i = 0; i < 4; ++i) {
      float d = dot2(t.k[i][0], q[g][0], 0.f);
      d = dot2(t.k[i][1], q[g][1], d);
      d = dot2(t.k[i][2], q[g][2], d);
      d = dot2(t.k[i][3], q[g][3], d);
      s[i] = d;
    }
#pragma unroll
    for (int i = 0; i < 4; ++i) s[i] = row_sum16(s[i]) * C2;
    const float tm = fmaxf(fmaxf(s[0], s[1]), fmaxf(s[2], s[3]));
    if (__builtin_amdgcn_ballot_w64(tm > m[g]) != 0ull) {
      const float mn = fmaxf(m[g], tm);
      const float al = __builtin_amdgcn_exp2f(m[g] - mn);
      m[g] = mn; l[g] *= al;
#pragma unroll
      for (int j = 0; j < 4; ++j) acc[g][j] *= al;
    }
#pragma unroll
    for (int i = 0; i < 4; ++i) p[g][i] = __builtin_amdgcn_exp2f(s[i] - m[g]);
    l[g] += (p[g][0] + p[g][1]) + (p[g][2] + p[g][3]);
  }
#pragma unroll
  for (int i = 0; i < 4; ++i) {
#pragma unroll
    for (int j = 0; j < 4; ++j) {
      const f32x2 vf = {bflo(t.v[i][j]), bfhi(t.v[i][j])};
#pragma unroll
      for (int g = 0; g < G; ++g) {
        const f32x2 pp = {p[g][i], p[g][i]};
        acc[g][j] = __builtin_elementwise_fma(pp, vf, acc[g][j]);
      }
    }
  }
}

__device__ __forceinline__ void load_wo(u32x4 (&w)[18], gptr wrow, int lane) {
#pragma unroll
  for (int i = 0; i < 16; ++i) w[i] = ld16(wrow + i * (DM * 2), (unsigned)(lane * 16));
#pragma unroll
  for (int e = 0; e < 2; ++e) w[16 + e] = ld16(wrow + e * (8 * DM * 2) + 1024, (unsigned)((lane >> 3) * (DM * 2) + (lane & 7) * 16));
}

template <int G, int NW>
__device__ __forceinline__ void body(const Args& a, const int br, const int kvh, const int j, float* lds) {
  const int tid = threadIdx.x, lane = tid & 63, wave = __builtin_amdgcn_readfirstlane(tid >> 6);
  const int r = lane >> 4, c = lane & 15;
  const int b = wave & 1, wv = wave >> 1;
  constexpr int HKV = 32 / G, HW = WAVES / 2;
  gptr xp = (gptr)(br ? a.x[1] : a.x[0]);
  gptr wqp = (gptr)(br ? a.wq[1] : a.wq[0]);
  gptr wop = (gptr)(br ? a.wo[1] : a.wo[0]);
  gptr kp = (gptr)(br ? a.k[1] : a.k[0]) + (size_t)(b * HKV + kvh) * (SK * DH * 2);
  gptr vp = (gptr)(br ? a.v[1] : a.v[0]) + (size_t)(b * HKV + kvh) * (SK * DH * 2);
  const int t0 = (TILES * j) / NW, t1 = (TILES * (j + 1)) / NW;
  const unsigned loff = lane * 16;
  int* cnt = reinterpret_cast<int*>(lds + LDS_CNT);
  int t = t0 + wv;

  u32x4 q[G];
  Tile ta, tb;
  TS(0);
  if (tid < 2) cnt[tid] = t0 + HW;
  {
    unsigned xr[2][9];
#pragma unroll
    for (int bb = 0; bb < 2; ++bb)
#pragma unroll
      for (int i = 0; i < 9; ++i)
        xr[bb][i] = *(const __attribute__((address_space(1))) unsigned*)(xp + bb * (DM * 2) + wave * 144 + i * 4 + (unsigned)(r * 36));
    u32x4 w[G][18];
#pragma unroll
    for (int g = 0; g < G; ++g)
#pragma unroll
      for (int i = 0; i < 18; ++i)
        w[g][i] = ld16(wqp + (size_t)(wave * 72 + i) * (NQ * 2) + (kvh * G + g) * (DH * 2), (unsigned)(r * 18 * NQ * 2 + c * 16));
    __builtin_amdgcn_sched_barrier(0);
    __builtin_amdgcn_s_barrier();
    load_tile(ta, kp + (size_t)t * 4096, vp + (size_t)t * 4096, loff);
    __builtin_amdgcn_sched_barrier(0);
#pragma unroll
    for (int g = 0; g < G; ++g) {
      f32x2 qa[2][4];
#pragma unroll
      for (int e = 0; e < 4; ++e) { qa[0][e] = f32x2{0.f, 0.f}; qa[1][e] = f32x2{0.f, 0.f}; }
#pragma unroll
      for (int i = 0; i < 18; ++i) {
        const float x0 = (i & 1) ? bfhi(xr[0][i >> 1]) : bflo(xr[0][i >> 1]);
        const float x1 = (i & 1) ? bfhi(xr[1][i >> 1]) : bflo(xr[1][i >> 1]);
#pragma unroll
        for (int e = 0; e < 4; ++e) {
          const f32x2 wf = {bflo(w[g][i][e]), bfhi(w[g][i][e])};
          qa[0][e] = __builtin_elementwise_fma(f32x2{x0, x0}, wf, qa[0][e]);
          qa[1][e] = __builtin_elementwise_fma(f32x2{x1, x1}, wf, qa[1][e]);
        }
        __builtin_amdgcn_sched_barrier(0);
      }
      float h[8];
#pragma unroll
      for (int e = 0; e < 8; ++e) {
        auto s = __builtin_amdgcn_permlane32_swap(__float_as_uint(qa[0][e >> 1][e & 1]), __float_as_uint(qa[1][e >> 1][e & 1]), false, false);
        h[e] = __uint_as_float(s[0]) + __uint_as_float(s[1]);
      }
      f32x4 o4;
#pragma unroll
      for (int e = 0; e < 4; ++e) {
        auto s = __builtin_amdgcn_permlane16_swap(__float_as_uint(h[e]), __float_as_uint(h[e + 4]), false, false);
        o4[e] = __uint_as_float(s[0]) + __uint_as_float(s[1]);
      }
      *reinterpret_cast<f32x4*>(lds + ((wave * G + g) * 2 + (r >> 1)) * DH + c * 8 + (r & 1) * 4) = o4;
    }
    __syncthreads();
#pragma unroll
    for (int g = 0; g < G; ++g) {
      f32x4 lo = {0.f, 0.f, 0.f, 0.f}, hi = {0.f, 0.f, 0.f, 0.f};
#pragma unroll
      for (int w2 = 0; w2 < WAVES; ++w2) {
        lo += *reinterpret_cast<const f32x4*>(lds + ((w2 * G + g) * 2 + b) * DH + c * 8);
        hi += *reinterpret_cast<const f32x4*>(lds + ((w2 * G + g) * 2 + b) * DH + c * 8 + 4);
      }
      q[g] = u32x4{cvtpk(lo[0], lo[1]), cvtpk(lo[2], lo[3]), cvtpk(hi[0], hi[1]), cvtpk(hi[2], hi[3])};
    }
  }
  u32x4 wo[G][18];
  if (G == 1) load_wo(wo[0], wop + (size_t)(kvh * DH + wave * 16) * (DM * 2), lane);

  TS(1);
  float m[G], l[G];
  f32x2 acc[G][4];
#pragma unroll
  for (int g = 0; g < G; ++g) { m[g] = -INFINITY; l[g] = 0.f;
#pragma unroll
    for (int e = 0; e < 4; ++e) acc[g][e] = f32x2{0.f, 0.f}; }
#define NEXT() __builtin_amdgcn_readfirstlane(lane == 0 ? atomicAdd(cnt + b, 1) : 0)
  while (true) {
    t = NEXT();
    if (t >= t1) { compute_tile<G>(ta, q, m, l, acc); break; }
    load_tile(tb, kp + (size_t)t * 4096, vp + (size_t)t * 4096, loff);
    compute_tile<G>(ta, q, m, l, acc);
    t = NEXT();
    if (t >= t1) { compute_tile<G>(tb, q, m, l, acc); break; }
    load_tile(ta, kp + (size_t)t * 4096, vp + (size_t)t * 4096, loff);
    compute_tile<G>(tb, q, m, l, acc);
  }
  TS(2);
  if (G == 2) {
#pragma unroll
    for (int g = 0; g < G; ++g) load_wo(wo[g], wop + (size_t)((kvh * G + g) * DH + wave * 16) * (DM * 2), lane);
  }

  float* rec = lds + LDS_REC;
  float* mlw = lds + LDS_ML;
#pragma unroll
  for (int g = 0; g < G; ++g) {
    const float M = xrow_max(m[g]);
    const float sc = __builtin_amdgcn_exp2f(m[g] - M);
    const float L = xrow_sum(l[g] * sc);
    float h[4];
#pragma unroll
    for (int e = 0; e < 4; ++e) {
      const float lo = acc[g][e >> 1][e & 1] * sc, hi = acc[g][2 + (e >> 1)][e & 1] * sc;
      auto s = __builtin_amdgcn_permlane32_swap(__float_as_uint(lo), __float_as_uint(hi), false, false);
      h[e] = __uint_as_float(s[0]) + __uint_as_float(s[1]);
    }
    f32x2 o2;
#pragma unroll
    for (int e = 0; e < 2; ++e) {
      auto s = __builtin_amdgcn_permlane16_swap(__float_as_uint(h[e]), __float_as_uint(h[e + 2]), false, false);
      o2[e] = __uint_as_float(s[0]) + __uint_as_float(s[1]);
    }
    *reinterpret_cast<f32x2*>(rec + (wave * G + g) * DH + c * 8 + (r >> 1) * 4 + (r & 1) * 2) = o2;
    if (lane == 0) *reinterpret_cast<f32x2*>(mlw + (wave * G + g) * 2) = f32x2{M, L};
  }
  __syncthreads();
  TS(3);
  float* yl = lds + LDS_Y;
  f32x2 mlout[G];
#pragma unroll
  for (int g = 0; g < G; ++g) {
    const int bb = r & 1;
    f32x2 mw[HW];
#pragma unroll
    for (int w2 = 0; w2 < HW; ++w2) mw[w2] = *reinterpret_cast<const f32x2*>(mlw + ((w2 * 2 + bb) * G + g) * 2);
    float M = mw[0][0];
#pragma unroll
    for (int w2 = 1; w2 < HW; ++w2) M = fmaxf(M, mw[w2][0]);
    float L = 0.f, o = 0.f;
#pragma unroll
    for (int w2 = 0; w2 < HW; ++w2) {
      const float wt = __builtin_amdgcn_exp2f(mw[w2][0] - M);
      L = fmaf(wt, mw[w2][1], L);
      o = fmaf(wt, rec[((w2 * 2 + bb) * G + g) * DH + wave * 16 + c], o);
    }
    mlout[g] = f32x2{M, L};
    f32x2 y[2][4], ye[2][4];
#pragma unroll
    for (int e = 0; e < 4; ++e) { y[0][e] = f32x2{0.f, 0.f}; y[1][e] = f32x2{0.f, 0.f}; ye[0][e] = f32x2{0.f, 0.f}; ye[1][e] = f32x2{0.f, 0.f}; }
#pragma unroll
    for (int i = 0; i < 16; ++i) {
      const float o0 = __uint_as_float(__builtin_amdgcn_readlane(__float_as_uint(o), i));
      const float o1 = __uint_as_float(__builtin_amdgcn_readlane(__float_as_uint(o), 16 + i));
#pragma unroll
      for (int e = 0; e < 4; ++e) {
        const f32x2 wf = {bflo(wo[g][i][e]), bfhi(wo[g][i][e])};
        y[0][e] = __builtin_elementwise_fma(f32x2{o0, o0}, wf, y[0][e]);
        y[1][e] = __builtin_elementwise_fma(f32x2{o1, o1}, wf, y[1][e]);
      }
    }
#pragma unroll
    for (int e2 = 0; e2 < 2; ++e2) {
      const int src = e2 * 8 + (lane >> 3);
      const float o0 = __uint_as_float(__builtin_amdgcn_ds_bpermute(src * 4, __float_as_uint(o)));
      const float o1 = __uint_as_float(__builtin_amdgcn_ds_bpermute((16 + src) * 4, __float_as_uint(o)));
#pragma unroll
      for (int e = 0; e < 4; ++e) {
        const f32x2 wf = {bflo(wo[g][16 + e2][e]), bfhi(wo[g][16 + e2][e])};
        ye[0][e] = __builtin_elementwise_fma(f32x2{o0, o0}, wf, ye[0][e]);
        ye[1][e] = __builtin_elementwise_fma(f32x2{o1, o1}, wf, ye[1][e]);
      }
    }
    float h[8];
#pragma unroll
    for (int e = 0; e < 8; ++e) {
      auto s = __builtin_amdgcn_permlane32_swap(__float_as_uint(ye[0][e >> 1][e & 1]), __float_as_uint(ye[1][e >> 1][e & 1]), false, false);
      h[e] = __uint_as_float(s[0]) + __uint_as_float(s[1]);
    }
    f32x4 e4;
#pragma unroll
    for (int e = 0; e < 4; ++e) {
      auto s = __builtin_amdgcn_permlane16_swap(__float_as_uint(h[e]), __float_as_uint(h[e + 4]), false, false);
      const float v = __uint_as_float(s[0]) + __uint_as_float(s[1]);
      e4[e] = v + dpp<XOR8>(v);
    }
    float* yw = yl + (g * WAVES + wave) * 2 * DM;
#pragma unroll
    for (int bb2 = 0; bb2 < 2; ++bb2) {
      *reinterpret_cast<f32x4*>(yw + bb2 * DM + lane * 8) = f32x4{y[bb2][0][0], y[bb2][0][1], y[bb2][1][0], y[bb2][1][1]};
      *reinterpret_cast<f32x4*>(yw + bb2 * DM + lane * 8 + 4) = f32x4{y[bb2][2][0], y[bb2][2][1], y[bb2][3][0], y[bb2][3][1]};
    }
    if ((lane & 8) == 0) *reinterpret_cast<f32x4*>(yw + (r >> 1) * DM + 512 + (lane & 7) * 8 + (r & 1) * 4) = e4;
  }
  __syncthreads();
  float* yws = a.ws + WS_Y / 4;
  for (int u = tid; u < G * 288; u += NTHR) {
    const int g = u / 288, bb = (u % 288) / 144, q4 = u % 144;
    f32x4 s = {0.f, 0.f, 0.f, 0.f};
#pragma unroll
    for (int w2 = 0; w2 < WAVES; ++w2) s += *reinterpret_cast<const f32x4*>(yl + ((g * WAVES + w2) * 2 + bb) * DM + q4 * 4);
    *reinterpret_cast<f32x4*>(yws + ((size_t)((br * 2 + bb) * 32 + kvh * G + g) * SMAX + j) * DM + q4 * 4) = s;
  }
  if (wave == 7 && (lane & 47) == 0) {
    f32x2* ml = reinterpret_cast<f32x2*>(a.ws + WS_ML / 4);
#pragma unroll
    for (int g = 0; g < G; ++g) ml[((br * 2 + (r & 1)) * 32 + kvh * G + g) * SMAX + j] = mlout[g];
  }
  TS(4);
}

__global__ void __launch_bounds__(NTHR, 2) attn_main(Args a) {
  __shared__ __attribute__((aligned(16))) float lds[LDS_FLOATS];
  const int xcd = blockIdx.x & 7, slot = blockIdx.x >> 3;
  if (slot < A_PER_X) body<2, NWA>(a, 0, ((xcd + AOFF) & 7) * 2 + slot / NWA, slot % NWA, lds);
  else body<1, NWB>(a, 1, xcd * 4 + (slot - A_PER_X) / NWB, (slot - A_PER_X) % NWB, lds);
}

__global__ void __launch_bounds__(256) attn_final(const float* ws, __hip_bfloat16* out) {
  __shared__ __attribute__((aligned(16))) float red[64 * 16];
  const int chunk = blockIdx.x % 36, bb = (blockIdx.x / 36) & 1, br = blockIdx.x / 72;
  const int tid = threadIdx.x, q4 = tid & 3, vi = tid >> 2;
  const int h = vi >> 1, half = vi & 1;
  const int S = br ? NWB : NWA;
  const f32x2* ml = reinterpret_cast<const f32x2*>(ws + WS_ML / 4) + ((br * 2 + bb) * 32 + h) * SMAX;
  const float* y = ws + WS_Y / 4 + (size_t)(((br * 2 + bb) * 32 + h) * SMAX) * DM + chunk * 16 + q4 * 4;
  f32x4 yv[4];
#pragma unroll
  for (int i = 0; i < 4; ++i) { const int s = half * 4 + i; yv[i] = s < S ? *reinterpret_cast<const f32x4*>(y + s * DM) : f32x4{0.f, 0.f, 0.f, 0.f}; }
  f32x2 mv[SMAX];
#pragma unroll
  for (int s = 0; s < SMAX; ++s) mv[s] = s < S ? ml[s] : f32x2{-INFINITY, 0.f};
  float M = -INFINITY;
#pragma unroll
  for (int s = 0; s < SMAX; ++s) M = fmaxf(M, mv[s][0]);
  float L = 0.f, wt[SMAX];
#pragma unroll
  for (int s = 0; s < SMAX; ++s) { wt[s] = __builtin_amdgcn_exp2f(mv[s][0] - M); L = fmaf(wt[s], mv[s][1], L); }
  const float inv = 1.f / L;
  f32x4 sacc = {0.f, 0.f, 0.f, 0.f};
#pragma unroll
  for (int i = 0; i < 4; ++i) { const float w = (half ? wt[4 + i] : wt[i]) * inv; sacc += yv[i] * w; }
  *reinterpret_cast<f32x4*>(red + vi * 16 + q4 * 4) = sacc;
  __syncthreads();
  if (tid < 64) {
    const int col = tid & 15, part = tid >> 4;
    float s = 0.f;
#pragma unroll
    for (int i = 0; i < 16; ++i) s += red[(part * 16 + i) * 16 + col];
    s = xrow_sum(s);
    if (tid < 16) out[bb * (2 * DM) + br * DM + chunk * 16 + col] = __float2bfloat16(s);
  }
}

extern "C" void kernel_launch(void* const* d_in, const int* in_sizes, int n_in,
                              void* d_out, int out_size, void* d_ws, size_t ws_size,
                              hipStream_t stream) {
  Args a;
  for (int i = 0; i < 2; ++i) {
    a.x[i] = (const unsigned short*)d_in[5 * i + 0];
    a.k[i] = (const unsigned short*)d_in[5 * i + 1];
    a.v[i] = (const unsigned short*)d_in[5 * i + 2];
    a.wq[i] = (const unsigned short*)d_in[5 * i + 3];
    a.wo[i] = (const unsigned short*)d_in[5 * i + 4];
  }
  a.ws = (float*)d_ws;
  hipLaunchKernelGGL(attn_main, dim3(GRID1), dim3(NTHR), 0, stream, a);
  hipLaunchKernelGGL(attn_final, dim3(144), dim3(256), 0, stream, (const float*)d_ws, (__hip_bfloat16*)d_out);
}
```

```cpp
#include <hip/hip_runtime.h>
#include <hip/hip_bf16.h>
#include <stdint.h>

typedef __attribute__((ext_vector_type(4))) unsigned u32x4;
typedef __attribute__((ext_vector_type(2))) unsigned u32x2;
typedef __attribute__((ext_vector_type(4))) float f32x4;
typedef __attribute__((ext_vector_type(2))) float f32x2;
typedef __attribute__((ext_vector_type(2))) __bf16 bf16x2;
typedef __attribute__((ext_vector_type(8))) __bf16 bf16x8;
typedef __attribute__((ext_vector_type(4))) short s16x4;

#define NWA 6
#define NWB 5
#define KV_NT 1
#define QBAR 1
#define WNT 1
#define PF2 1
#define BIAS 0
#define HT 102
#define AOFF 1

#define TS(i) do {} while (0)
constexpr int DM = 576, DH = 128, SK = 8192, NQ = 4096;
constexpr int WAVES = 8, NTHR = WAVES * 64;
constexpr int TILES = SK / 16;
constexpr int SMAX = 8;
constexpr int A_PER_X = 2 * NWA, B_PER_X = 4 * NWB, WG_PER_X = A_PER_X + B_PER_X;
constexpr int GRID1 = 8 * WG_PER_X;
constexpr float C2 = 0.088388347648318440f * 1.4426950408889634f;

constexpr size_t WS_ML = 0, WS_Y = 65536;
constexpr int RECS = 272;
constexpr int LDS_Q = 131072, LDS_REC = LDS_Q + 16384, LDS_ML = LDS_REC + 2 * 16 * RECS, LDS_BYTES = LDS_ML + 128;

struct Args {
  const unsigned short* x[2];
  const unsigned short* k[2];
  const unsigned short* v[2];
  const unsigned short* wq[2];
  const unsigned short* wo[2];
  float* ws;
};

__device__ __forceinline__ float dot2(unsigned a, unsigned b, float acc) {
  return __builtin_amdgcn_fdot2_f32_bf16(__builtin_bit_cast(bf16x2, a), __builtin_bit_cast(bf16x2, b), acc, false);
}
__device__ __forceinline__ unsigned cvtpk(float lo, float hi) {
  unsigned r; asm("v_cvt_pk_bf16_f32 %0, %1, %2" : "=v"(r) : "v"(lo), "v"(hi)); return r;
}
template <int CTRL> __device__ __forceinline__ float dpp(float x) {
  return __builtin_bit_cast(float, __builtin_amdgcn_mov_dpp(__builtin_bit_cast(int, x), CTRL, 0xf, 0xf, true));
}
constexpr int XOR1 = 0xB1, XOR2 = 0x4E, XOR7 = 0x141, XOR8 = 0x128;
__device__ __forceinline__ float row_sum16(float x) {
  x += dpp<XOR8>(x); x += dpp<XOR7>(x); x += dpp<XOR2>(x); x += dpp<XOR1>(x); return x;
}
__device__ __forceinline__ float xrow_max(float x) {
  auto s = __builtin_amdgcn_permlane16_swap(__float_as_uint(x), __float_as_uint(x), false, false);
  x = fmaxf(__uint_as_float(s[0]), __uint_as_float(s[1]));
  auto t = __builtin_amdgcn_permlane32_swap(__float_as_uint(x), __float_as_uint(x), false, false);
  return fmaxf(__uint_as_float(t[0]), __uint_as_float(t[1]));
}
__device__ __forceinline__ float xrow_sum(float x) {
  auto s = __builtin_amdgcn_permlane16_swap(__float_as_uint(x), __float_as_uint(x), false, false);
  x = __uint_as_float(s[0]) + __uint_as_float(s[1]);
  auto t = __builtin_amdgcn_permlane32_swap(__float_as_uint(x), __float_as_uint(x), false, false);
  return __uint_as_float(t[0]) + __uint_as_float(t[1]);
}
__device__ __forceinline__ float bflo(unsigned u) { return __uint_as_float(u << 16); }
__device__ __forceinline__ float bfhi(unsigned u) { return __uint_as_float(u & 0xffff0000u); }
typedef const __attribute__((address_space(1))) char* gptr;
typedef const __attribute__((address_space(1))) u32x4* gvec;
__device__ __forceinline__ u32x4 ldkv(gptr base, unsigned off) {
  return __builtin_nontemporal_load((gvec)(base + off));
}
__device__ __forceinline__ u32x4 ld16(gptr base, unsigned off) {
  return __builtin_nontemporal_load((gvec)(base + off));
}

struct Tile { u32x4 k[4]; u32x4 v[4]; };

__device__ __forceinline__ void load_tile(Tile& t, gptr kp, gptr vp, unsigned loff) {
#pragma unroll
  for (int i = 0; i < 4; ++i) t.k[i] = ldkv(kp + i * 1024, loff);
#pragma unroll
  for (int i = 0; i < 4; ++i) t.v[i] = ldkv(vp + i * 1024, loff);
}

template <int G>
__device__ __forceinline__ void compute_tile(const Tile& t, const u32x4 (&q)[G], float (&m)[G], float (&l)[G], f32x2 (&acc)[G][4]) {
  float p[G][4];
#pragma unroll
  for (int g = 0; g < G; ++g) {
    float s[4];
#pragma unroll
    for (int i = 0; i < 4; ++i) {
      float d = dot2(t.k[i][0], q[g][0], 0.f);
      d = dot2(t.k[i][1], q[g][1], d);
      d = dot2(t.k[i][2], q[g][2], d);
      d = dot2(t.k[i][3], q[g][3], d);
      s[i] = d;
    }
#pragma unroll
    for (int i = 0; i < 4; ++i) s[i] = row_sum16(s[i]) * C2;
    const float tm = fmaxf(fmaxf(s[0], s[1]), fmaxf(s[2], s[3]));
    if (__builtin_amdgcn_ballot_w64(tm > m[g]) != 0ull) {
      const float mn = fmaxf(m[g], tm);
      const float al = __builtin_amdgcn_exp2f(m[g] - mn);
      m[g] = mn; l[g] *= al;
#pragma unroll
      for (int j = 0; j < 4; ++j) acc[g][j] *= al;
    }
#pragma unroll
    for (int i = 0; i < 4; ++i) p[g][i] = __builtin_amdgcn_exp2f(s[i] - m[g]);
    l[g] += (p[g][0] + p[g][1]) + (p[g][2] + p[g][3]);
  }
#pragma unroll
  for (int i = 0; i < 4; ++i) {
#pragma unroll
    for (int j = 0; j < 4; ++j) {
      const f32x2 vf = {bflo(t.v[i][j]), bfhi(t.v[i][j])};
#pragma unroll
      for (int g = 0; g < G; ++g) {
        const f32x2 pp = {p[g][i], p[g][i]};
        acc[g][j] = __builtin_elementwise_fma(pp, vf, acc[g][j]);
      }
    }
  }
}

#define LBAR() asm volatile("s_waitcnt lgkmcnt(0)\n\ts_barrier" ::: "memory")
__device__ __forceinline__ f32x4 mfma32(u32x4 a, u32x4 b, f32x4 c) {
  return __builtin_amdgcn_mfma_f32_16x16x32_bf16(__builtin_bit_cast(bf16x8, a), __builtin_bit_cast(bf16x8, b), c, 0, 0, 0);
}
__device__ __forceinline__ f32x4 mfma16(u32x2 a, u32x2 b, f32x4 c) {
  return __builtin_amdgcn_mfma_f32_16x16x16bf16_1k(__builtin_bit_cast(s16x4, a), __builtin_bit_cast(s16x4, b), c, 0, 0, 0);
}
__device__ __forceinline__ unsigned pairk(unsigned r0, unsigned r1, int odd) {
  return __builtin_amdgcn_perm(r1, r0, odd ? 0x07060302u : 0x05040100u);
}
__device__ __forceinline__ void mm8(f32x4 (&d)[8], const u32x4 a, const u32x4* w) {
#pragma unroll
  for (int j = 0; j < 8; ++j) {
    const u32x4 bv = {pairk(w[0][j >> 1], w[1][j >> 1], j & 1), pairk(w[2][j >> 1], w[3][j >> 1], j & 1),
                      pairk(w[4][j >> 1], w[5][j >> 1], j & 1), pairk(w[6][j >> 1], w[7][j >> 1], j & 1)};
    d[j] = mfma32(a, bv, d[j]);
  }
}

template <int G>
__device__ __forceinline__ void body(const Args& a, const int br, const int kvh, const int j, const int t0, const int t1, char* lds) {
  const int tid = threadIdx.x, lane = tid & 63, wave = __builtin_amdgcn_readfirstlane(tid >> 6);
  const int r = lane >> 4, c = lane & 15;
  const int b = wave & 1, wv = wave >> 1;
  constexpr int HKV = 32 / G, HW = WAVES / 2;
  gptr xp = (gptr)(br ? a.x[1] : a.x[0]);
  gptr wqp = (gptr)(br ? a.wq[1] : a.wq[0]);
  gptr wop = (gptr)(br ? a.wo[1] : a.wo[0]);
  gptr kp = (gptr)(br ? a.k[1] : a.k[0]) + (size_t)(b * HKV + kvh) * (SK * DH * 2);
  gptr vp = (gptr)(br ? a.v[1] : a.v[0]) + (size_t)(b * HKV + kvh) * (SK * DH * 2);
  const unsigned loff = lane * 16;
  int t = t0 + wv;

  u32x4 q[G];
  Tile ta, tb;
  TS(0);
  {
    unsigned xr[9];
#pragma unroll
    for (int i = 0; i < 9; ++i)
      xr[i] = *(const __attribute__((address_space(1))) unsigned*)(xp + wave * 144 + i * 4 + (unsigned)((c & 1) * (DM * 2) + r * 36));
    u32x4 w[G][18];
#pragma unroll
    for (int g = 0; g < G; ++g)
#pragma unroll
      for (int i = 0; i < 18; ++i)
        w[g][i] = ld16(wqp + (size_t)(wave * 72 + i) * (NQ * 2) + (kvh * G + g) * (DH * 2), (unsigned)(r * 18 * NQ * 2 + c * 16));
    __builtin_amdgcn_sched_barrier(0);
    __builtin_amdgcn_s_barrier();
    load_tile(ta, kp + (size_t)t * 4096, vp + (size_t)t * 4096, loff);
    if (PF2 == 2 || (PF2 == 1 && G == 1)) load_tile(tb, kp + (size_t)(t + HW) * 4096, vp + (size_t)(t + HW) * 4096, loff);
    __builtin_amdgcn_sched_barrier(0);
    float* qp = reinterpret_cast<float*>(lds + LDS_Q);
#pragma unroll
    for (int g = 0; g < G; ++g) {
      f32x4 d[8];
#pragma unroll
      for (int e = 0; e < 8; ++e) d[e] = f32x4{0.f, 0.f, 0.f, 0.f};
      mm8(d, u32x4{xr[0], xr[1], xr[2], xr[3]}, &w[g][0]);
      __builtin_amdgcn_sched_barrier(0);
      mm8(d, u32x4{xr[4], xr[5], xr[6], xr[7]}, &w[g][8]);
      __builtin_amdgcn_sched_barrier(0);
#pragma unroll
      for (int e = 0; e < 8; ++e) d[e] = mfma16(u32x2{xr[8], 0u}, u32x2{pairk(w[g][16][e >> 1], w[g][17][e >> 1], e & 1), 0u}, d[e]);
      f32x4 o4;
#pragma unroll
      for (int e = 0; e < 4; ++e) {
        const float lo = (r & 2) ? d[e][1] : d[e][0], hi = (r & 2) ? d[e + 4][1] : d[e + 4][0];
        o4[e] = (r & 1) ? hi : lo;
      }
      *reinterpret_cast<f32x4*>(qp + ((wave * G + g) * 2 + (r >> 1)) * DH + c * 8 + (r & 1) * 4) = o4;
    }
    LBAR();
#pragma unroll
    for (int g = 0; g < G; ++g) {
      f32x4 lo = {0.f, 0.f, 0.f, 0.f}, hi = {0.f, 0.f, 0.f, 0.f};
#pragma unroll
      for (int w2 = 0; w2 < WAVES; ++w2) {
        lo += *reinterpret_cast<const f32x4*>(qp + ((w2 * G + g) * 2 + b) * DH + c * 8);
        hi += *reinterpret_cast<const f32x4*>(qp + ((w2 * G + g) * 2 + b) * DH + c * 8 + 4);
      }
      q[g] = u32x4{cvtpk(lo[0], lo[1]), cvtpk(lo[2], lo[3]), cvtpk(hi[0], hi[1]), cvtpk(hi[2], hi[3])};
    }
  }
  __builtin_amdgcn_sched_barrier(0);
  u32x4 wm[16];
  u32x2 wsd[G][4];
  {
    gptr wh = wop + (size_t)(kvh * G * DH) * (DM * 2);
    const unsigned mo = (unsigned)((b * 64 + r * 8) * (DM * 2) + wv * 256 + c * 16);
    const unsigned so = (unsigned)((wave * 16 + r * 4) * (DM * 2) + 1024 + c * 8);
#pragma unroll
    for (int i = 0; i < 16; ++i) wm[i] = ld16(wh + ((i >> 3) * 32 + (i & 7)) * (DM * 2), mo);
#pragma unroll
    for (int g = 0; g < G; ++g)
#pragma unroll
      for (int i = 0; i < 4; ++i)
        wsd[g][i] = __builtin_nontemporal_load((const __attribute__((address_space(1))) u32x2*)(wh + (g * DH + i) * (DM * 2) + so));
    if (G == 2) {
      const unsigned lb = (unsigned)(size_t)(__attribute__((address_space(3))) char*)lds + wave * 16384;
#pragma unroll
      for (int i = 0; i < 16; ++i)
        asm volatile("s_mov_b32 m0, %0\n\ts_nop 0\n\tglobal_load_lds_dwordx4 %1, %2 nt"
                     :: "s"(lb + i * 1024), "v"(mo), "s"(wh + (DH + (i >> 3) * 32 + (i & 7)) * (DM * 2)) : "m0", "memory");
    }
  }
  __builtin_amdgcn_sched_barrier(0);

  TS(1);
  float m[G], l[G];
  f32x2 acc[G][4];
#pragma unroll
  for (int g = 0; g < G; ++g) { m[g] = -INFINITY; l[g] = 0.f;
#pragma unroll
    for (int e = 0; e < 4; ++e) acc[g][e] = f32x2{0.f, 0.f}; }
  if (!(PF2 == 2 || (PF2 == 1 && G == 1))) load_tile(tb, kp + (size_t)(t + HW) * 4096, vp + (size_t)(t + HW) * 4096, loff);
  t += 2 * HW;
#define SB() __builtin_amdgcn_sched_barrier(0)
  for (; t + HW < t1; t += 2 * HW) {
    compute_tile<G>(ta, q, m, l, acc); SB();
    load_tile(ta, kp + (size_t)t * 4096, vp + (size_t)t * 4096, loff); SB();
    compute_tile<G>(tb, q, m, l, acc); SB();
    load_tile(tb, kp + (size_t)(t + HW) * 4096, vp + (size_t)(t + HW) * 4096, loff); SB();
  }
  compute_tile<G>(ta, q, m, l, acc); SB();
  const bool more = t < t1;
  if (more) load_tile(ta, kp + (size_t)t * 4096, vp + (size_t)t * 4096, loff);
  SB();
  compute_tile<G>(tb, q, m, l, acc); SB();
  if (more) compute_tile<G>(ta, q, m, l, acc);
  TS(2);
  u32x4 wm2[G == 2 ? 16 : 1];
  if (G == 2) {
    asm volatile("s_waitcnt vmcnt(0)" ::: "memory");
#pragma unroll
    for (int i = 0; i < 16; ++i) wm2[i] = *reinterpret_cast<const u32x4*>(lds + wave * 16384 + i * 1024 + lane * 16);
  }

  f32x2* mlw = reinterpret_cast<f32x2*>(lds + LDS_ML);
#pragma unroll
  for (int g = 0; g < G; ++g) {
    const float M = xrow_max(m[g]);
    const float sc = __builtin_amdgcn_exp2f(m[g] - M);
    const float L = xrow_sum(l[g] * sc);
    float h[4];
#pragma unroll
    for (int e = 0; e < 4; ++e) {
      const float lo = acc[g][e >> 1][e & 1] * sc, hi = acc[g][2 + (e >> 1)][e & 1] * sc;
      auto s = __builtin_amdgcn_permlane32_swap(__float_as_uint(lo), __float_as_uint(hi), false, false);
      h[e] = __uint_as_float(s[0]) + __uint_as_float(s[1]);
    }
    f32x2 o2;
#pragma unroll
    for (int e = 0; e < 2; ++e) {
      auto s = __builtin_amdgcn_permlane16_swap(__float_as_uint(h[e]), __float_as_uint(h[e + 2]), false, false);
      o2[e] = __uint_as_float(s[0]) + __uint_as_float(s[1]);
    }
    const unsigned ph = cvtpk(o2[0], o2[1]);
    const unsigned pl = cvtpk(o2[0] - bflo(ph), o2[1] - bfhi(ph));
    char* rp = lds + LDS_REC + (g * 16 + wv * 4 + b) * RECS + c * 16 + r * 4;
    *reinterpret_cast<unsigned*>(rp) = ph;
    *reinterpret_cast<unsigned*>(rp + 2 * RECS) = pl;
    if (lane == 0) mlw[wave * G + g] = f32x2{M, L};
  }
  LBAR();
  TS(3);
  float* yl = reinterpret_cast<float*>(lds + LDS_Q);
  f32x4 ym[G];
  f32x2 mlout[G];
#pragma unroll
  for (int g = 0; g < G; ++g) {
    const char* rp = lds + LDS_REC + (g * 16 + c) * RECS;
    const u32x4 a0 = *reinterpret_cast<const u32x4*>(rp + b * 128 + r * 16);
    const u32x4 a1 = *reinterpret_cast<const u32x4*>(rp + b * 128 + 64 + r * 16);
    const u32x2 a2 = *reinterpret_cast<const u32x2*>(rp + wave * 32 + r * 8);
    const f32x2 ml0 = mlw[(r * 2) * G + g], ml1 = mlw[(r * 2 + 1) * G + g];
    f32x4 d[8], ds[4];
#pragma unroll
    for (int e = 0; e < 8; ++e) d[e] = f32x4{0.f, 0.f, 0.f, 0.f};
    mm8(d, a0, g ? &wm2[0] : &wm[0]);
    mm8(d, a1, g ? &wm2[G == 2 ? 8 : 0] : &wm[8]);
#pragma unroll
    for (int e = 0; e < 4; ++e)
      ds[e] = mfma16(a2, u32x2{pairk(wsd[g][0][e >> 1], wsd[g][1][e >> 1], e & 1), pairk(wsd[g][2][e >> 1], wsd[g][3][e >> 1], e & 1)},
                     f32x4{0.f, 0.f, 0.f, 0.f});
    const float M0 = xrow_max(ml0[0]), M1 = xrow_max(ml1[0]);
    const float w0 = __builtin_amdgcn_exp2f(ml0[0] - M0), w1 = __builtin_amdgcn_exp2f(ml1[0] - M1);
    const float L0 = xrow_sum(w0 * ml0[1]), L1 = xrow_sum(w1 * ml1[1]);
    mlout[g] = (r & 1) ? f32x2{M1, L1} : f32x2{M0, L0};
    float h[8], hs[4];
#pragma unroll
    for (int e = 0; e < 8; ++e) {
      auto s = __builtin_amdgcn_permlane32_swap(__float_as_uint(w0 * (d[e][0] + d[e][2])), __float_as_uint(w1 * (d[e][1] + d[e][3])), false, false);
      h[e] = __uint_as_float(s[0]) + __uint_as_float(s[1]);
    }
#pragma unroll
    for (int e = 0; e < 4; ++e) {
      auto s = __builtin_amdgcn_permlane32_swap(__float_as_uint(w0 * (ds[e][0] + ds[e][2])), __float_as_uint(w1 * (ds[e][1] + ds[e][3])), false, false);
      hs[e] = __uint_as_float(s[0]) + __uint_as_float(s[1]);
    }
#pragma unroll
    for (int e = 0; e < 4; ++e) {
      auto s = __builtin_amdgcn_permlane16_swap(__float_as_uint(h[e]), __float_as_uint(h[e + 4]), false, false);
      ym[g][e] = __uint_as_float(s[0]) + __uint_as_float(s[1]);
    }
    f32x2 ys;
#pragma unroll
    for (int e = 0; e < 2; ++e) {
      auto s = __builtin_amdgcn_permlane16_swap(__float_as_uint(hs[e]), __float_as_uint(hs[e + 2]), false, false);
      ys[e] = __uint_as_float(s[0]) + __uint_as_float(s[1]);
    }
    if (b) *reinterpret_cast<f32x4*>(yl + ((g * 4 + wv) * 64 + lane) * 4) = ym[g];
    *reinterpret_cast<f32x2*>(yl + 2048 + ((g * 8 + wave) * 64 + lane) * 2) = ys;
  }
  LBAR();
  float* yws = a.ws + WS_Y / 4 + ((size_t)((br * 2 + (r >> 1)) * 32 + kvh * G) * SMAX + j) * DM;
  if (!b) {
#pragma unroll
    for (int g = 0; g < G; ++g)
      *reinterpret_cast<f32x4*>(yws + g * (SMAX * DM) + wv * 128 + c * 8 + (r & 1) * 4) =
          ym[g] + *reinterpret_cast<const f32x4*>(yl + ((g * 4 + wv) * 64 + lane) * 4);
  } else if (wv < G) {
    f32x2 s = {0.f, 0.f};
#pragma unroll
    for (int w2 = 0; w2 < WAVES; ++w2) s += *reinterpret_cast<const f32x2*>(yl + 2048 + ((wv * 8 + w2) * 64 + lane) * 2);
    *reinterpret_cast<f32x2*>(yws + wv * (SMAX * DM) + 512 + c * 4 + (r & 1) * 2) = s;
  } else if (wv == 3 && (lane & 47) == 0) {
    f32x2* ml = reinterpret_cast<f32x2*>(a.ws + WS_ML / 4);
#pragma unroll
    for (int g = 0; g < G; ++g) ml[((br * 2 + (r & 1)) * 32 + kvh * G + g) * SMAX + j] = mlout[g];
  }
  TS(4);
}

__global__ void __launch_bounds__(NTHR, 2) attn_main(Args a) {
  __shared__ __attribute__((aligned(16))) char lds[LDS_BYTES];
  const int xcd = blockIdx.x & 7, slot = blockIdx.x >> 3;
  int isa, h, j;
  isa = slot < A_PER_X;
  if (isa) { h = ((xcd + AOFF) & 7) * 2 + slot / NWA; j = slot % NWA; }
  else { h = xcd * 4 + (slot - A_PER_X) / NWB; j = (slot - A_PER_X) % NWB; }
  const bool sh = false;
  if (isa) body<2>(a, 0, h, j, (TILES * j) / NWA, (TILES * (j + 1)) / NWA, lds);
  else {
    const int t0 = sh ? ((TILES - HT) * j) / 4 : (TILES * j) / NWB;
    const int t1 = sh ? (j == 4 ? TILES : ((TILES - HT) * (j + 1)) / 4) : (TILES * (j + 1)) / NWB;
    body<1>(a, 1, h, j, t0, t1, lds);
  }
}
#define IS_B(i) (((i) >> 3) >= A_PER_X)

__global__ void __launch_bounds__(256) attn_final(const float* ws, __hip_bfloat16* out) {
  __shared__ __attribute__((aligned(16))) float red[64 * 16];
  const int chunk = blockIdx.x % 36, bb = (blockIdx.x / 36) & 1, br = blockIdx.x / 72;
  const int tid = threadIdx.x, q4 = tid & 3, vi = tid >> 2;
  const int h = vi >> 1, half = vi & 1;
  const int S = br ? NWB : NWA;
  const f32x2* ml = reinterpret_cast<const f32x2*>(ws + WS_ML / 4) + ((br * 2 + bb) * 32 + h) * SMAX;
  const float* y = ws + WS_Y / 4 + (size_t)(((br * 2 + bb) * 32 + h) * SMAX) * DM + chunk * 16 + q4 * 4;
  f32x4 yv[3];
  f32x2 mv[6];
#pragma unroll
  for (int i = 0; i < 3; ++i) { const int s = min(half * 3 + i, S - 1); yv[i] = *reinterpret_cast<const f32x4*>(y + s * DM); }
#pragma unroll
  for (int s = 0; s < 6; ++s) mv[s] = ml[min(s, S - 1)];
  if (S == 5) mv[5] = f32x2{-INFINITY, 0.f};
  float M = mv[0][0];
#pragma unroll
  for (int s = 1; s < 6; ++s) M = fmaxf(M, mv[s][0]);
  float L = 0.f, wt[6];
#pragma unroll
  for (int s = 0; s < 6; ++s) { wt[s] = __builtin_amdgcn_exp2f(mv[s][0] - M); L = fmaf(wt[s], mv[s][1], L); }
  const float inv = 1.f / L;
  const float w0 = half ? wt[3] : wt[0], w1 = half ? wt[4] : wt[1], w2 = half ? wt[5] : wt[2];
  const f32x4 sacc = (yv[0] * w0 + yv[1] * w1 + yv[2] * w2) * inv;
  *reinterpret_cast<f32x4*>(red + vi * 16 + q4 * 4) = sacc;
  __syncthreads();
  if (tid < 64) {
    const int col = tid & 15, part = tid >> 4;
    float s = 0.f;
#pragma unroll
    for (int i = 0; i < 16; ++i) s += red[(part * 16 + i) * 16 + col];
    s = xrow_sum(s);
    if (tid < 16) out[bb * (2 * DM) + br * DM + chunk * 16 + col] = __float2bfloat16(s);
  }
}

extern "C" void kernel_launch(void* const* d_in, const int* in_sizes, int n_in,
                              void* d_out, int out_size, void* d_ws, size_t ws_size,
                              hipStream_t stream) {
  Args a;
  for (int i = 0; i < 2; ++i) {
    a.x[i] = (const unsigned short*)d_in[5 * i + 0];
    a.k[i] = (const unsigned short*)d_in[5 * i + 1];
    a.v[i] = (const unsigned short*)d_in[5 * i + 2];
    a.wq[i] = (const unsigned short*)d_in[5 * i + 3];
    a.wo[i] = (const unsigned short*)d_in[5 * i + 4];
  }
  a.ws = (float*)d_ws;
  hipLaunchKernelGGL(attn_main, dim3(GRID1), dim3(NTHR), 0, stream, a);
  hipLaunchKernelGGL(attn_final, dim3(144), dim3(256), 0, stream, (const float*)d_ws, (__hip_bfloat16*)d_out);
}
```

```cpp
#include <hip/hip_runtime.h>
#include <hip/hip_bf16.h>
#include <stdint.h>

typedef __attribute__((ext_vector_type(4))) unsigned u32x4;
typedef __attribute__((ext_vector_type(2))) unsigned u32x2;
typedef __attribute__((ext_vector_type(4))) float f32x4;
typedef __attribute__((ext_vector_type(2))) float f32x2;
typedef __attribute__((ext_vector_type(2))) __bf16 bf16x2;
typedef __attribute__((ext_vector_type(8))) __bf16 bf16x8;
typedef __attribute__((ext_vector_type(4))) short s16x4;

#define NWA 6
#define NWB 5
#define KVAUX 2
#define QBAR 1
#define WNT 1
#define PF2 1
#define BIAS 0
#define HT 102
#define CPF 0
#define LOCK 0
#define AOFF 1

#define TS(i) do {} while (0)
constexpr int DM = 576, DH = 128, SK = 8192, NQ = 4096;
constexpr int WAVES = 8, NTHR = WAVES * 64;
constexpr int TILES = SK / 16;
constexpr int SMAX = 8;
constexpr int A_PER_X = 2 * NWA, B_PER_X = 4 * NWB, WG_PER_X = A_PER_X + B_PER_X;
constexpr int GRID1 = 8 * WG_PER_X;
constexpr float C2 = 0.088388347648318440f * 1.4426950408889634f;

constexpr size_t WS_ML = 0, WS_Y = 65536;
constexpr int RECS = 272;
constexpr int LDS_Q = 131072, LDS_REC = LDS_Q + 16384, LDS_ML = LDS_REC + 2 * 16 * RECS, LDS_BYTES = LDS_ML + 128;

struct Args {
  const unsigned short* x[2];
  const unsigned short* k[2];
  const unsigned short* v[2];
  const unsigned short* wq[2];
  const unsigned short* wo[2];
  float* ws;
};

__device__ __forceinline__ float dot2(unsigned a, unsigned b, float acc) {
  return __builtin_amdgcn_fdot2_f32_bf16(__builtin_bit_cast(bf16x2, a), __builtin_bit_cast(bf16x2, b), acc, false);
}
__device__ __forceinline__ unsigned cvtpk(float lo, float hi) {
  unsigned r; asm("v_cvt_pk_bf16_f32 %0, %1, %2" : "=v"(r) : "v"(lo), "v"(hi)); return r;
}
template <int CTRL> __device__ __forceinline__ float dpp(float x) {
  return __builtin_bit_cast(float, __builtin_amdgcn_mov_dpp(__builtin_bit_cast(int, x), CTRL, 0xf, 0xf, true));
}
constexpr int XOR1 = 0xB1, XOR2 = 0x4E, XOR7 = 0x141, XOR8 = 0x128;
__device__ __forceinline__ float row_sum16(float x) {
  x += dpp<XOR8>(x); x += dpp<XOR7>(x); x += dpp<XOR2>(x); x += dpp<XOR1>(x); return x;
}
__device__ __forceinline__ float xrow_max(float x) {
  auto s = __builtin_amdgcn_permlane16_swap(__float_as_uint(x), __float_as_uint(x), false, false);
  x = fmaxf(__uint_as_float(s[0]), __uint_as_float(s[1]));
  auto t = __builtin_amdgcn_permlane32_swap(__float_as_uint(x), __float_as_uint(x), false, false);
  return fmaxf(__uint_as_float(t[0]), __uint_as_float(t[1]));
}
__device__ __forceinline__ float xrow_sum(float x) {
  auto s = __builtin_amdgcn_permlane16_swap(__float_as_uint(x), __float_as_uint(x), false, false);
  x = __uint_as_float(s[0]) + __uint_as_float(s[1]);
  auto t = __builtin_amdgcn_permlane32_swap(__float_as_uint(x), __float_as_uint(x), false, false);
  return __uint_as_float(t[0]) + __uint_as_float(t[1]);
}
__device__ __forceinline__ float bflo(unsigned u) { return __uint_as_float(u << 16); }
__device__ __forceinline__ float bfhi(unsigned u) { return __uint_as_float(u & 0xffff0000u); }
typedef const __attribute__((address_space(1))) char* gptr;
typedef const __attribute__((address_space(1))) u32x4* gvec;
typedef __amdgpu_buffer_rsrc_t rsrc_t;
__device__ __forceinline__ rsrc_t mkrsrc(gptr p, unsigned bytes) {
  return __builtin_amdgcn_make_buffer_rsrc((void*)(const char*)p, (short)0, (int)bytes, 0x00020000);
}
__device__ __forceinline__ u32x4 ld16(gptr base, unsigned off) {
  return __builtin_nontemporal_load((gvec)(base + off));
}

struct Tile { u32x4 k[4]; u32x4 v[4]; };

__device__ __forceinline__ void load_tile(Tile& t, rsrc_t kr, rsrc_t vr, int tile, unsigned loff) {
#pragma unroll
  for (int i = 0; i < 4; ++i) t.k[i] = __builtin_amdgcn_raw_buffer_load_b128(kr, loff + i * 1024, tile * 4096, KVAUX);
#pragma unroll
  for (int i = 0; i < 4; ++i) t.v[i] = __builtin_amdgcn_raw_buffer_load_b128(vr, loff + i * 1024, tile * 4096, KVAUX);
}

template <int G>
__device__ __forceinline__ void compute_tile(const Tile& t, const u32x4 (&q)[G], float (&m)[G], float (&l)[G], f32x2 (&acc)[G][4]) {
  float p[G][4];
#pragma unroll
  for (int g = 0; g < G; ++g) {
    float s[4];
#pragma unroll
    for (int i = 0; i < 4; ++i) {
      float d = dot2(t.k[i][0], q[g][0], 0.f);
      d = dot2(t.k[i][1], q[g][1], d);
      d = dot2(t.k[i][2], q[g][2], d);
      d = dot2(t.k[i][3], q[g][3], d);
      s[i] = d;
    }
#pragma unroll
    for (int i = 0; i < 4; ++i) s[i] = row_sum16(s[i]) * C2;
    const float tm = fmaxf(fmaxf(s[0], s[1]), fmaxf(s[2], s[3]));
    if (__builtin_amdgcn_ballot_w64(tm > m[g]) != 0ull) {
      const float mn = fmaxf(m[g], tm);
      const float al = __builtin_amdgcn_exp2f(m[g] - mn);
      m[g] = mn; l[g] *= al;
#pragma unroll
      for (int j = 0; j < 4; ++j) acc[g][j] *= al;
    }
#pragma unroll
    for (int i = 0; i < 4; ++i) p[g][i] = __builtin_amdgcn_exp2f(s[i] - m[g]);
    l[g] += (p[g][0] + p[g][1]) + (p[g][2] + p[g][3]);
  }
#pragma unroll
  for (int i = 0; i < 4; ++i) {
#pragma unroll
    for (int j = 0; j < 4; ++j) {
      const f32x2 vf = {bflo(t.v[i][j]), bfhi(t.v[i][j])};
#pragma unroll
      for (int g = 0; g < G; ++g) {
        const f32x2 pp = {p[g][i], p[g][i]};
        acc[g][j] = __builtin_elementwise_fma(pp, vf, acc[g][j]);
      }
    }
  }
}

#define LBAR() asm volatile("s_waitcnt lgkmcnt(0)\n\ts_barrier" ::: "memory")
__device__ __forceinline__ f32x4 mfma32(u32x4 a, u32x4 b, f32x4 c) {
  return __builtin_amdgcn_mfma_f32_16x16x32_bf16(__builtin_bit_cast(bf16x8, a), __builtin_bit_cast(bf16x8, b), c, 0, 0, 0);
}
__device__ __forceinline__ f32x4 mfma16(u32x2 a, u32x2 b, f32x4 c) {
  return __builtin_amdgcn_mfma_f32_16x16x16bf16_1k(__builtin_bit_cast(s16x4, a), __builtin_bit_cast(s16x4, b), c, 0, 0, 0);
}
__device__ __forceinline__ unsigned pairk(unsigned r0, unsigned r1, int odd) {
  return __builtin_amdgcn_perm(r1, r0, odd ? 0x07060302u : 0x05040100u);
}
__device__ __forceinline__ void mm8(f32x4 (&d)[8], const u32x4 a, const u32x4* w) {
#pragma unroll
  for (int j = 0; j < 8; ++j) {
    const u32x4 bv = {pairk(w[0][j >> 1], w[1][j >> 1], j & 1), pairk(w[2][j >> 1], w[3][j >> 1], j & 1),
                      pairk(w[4][j >> 1], w[5][j >> 1], j & 1), pairk(w[6][j >> 1], w[7][j >> 1], j & 1)};
    d[j] = mfma32(a, bv, d[j]);
  }
}

template <int G>
__device__ __forceinline__ void body(const Args& a, const int br, const int kvh, const int j, const int t0, const int t1, char* lds) {
  const int tid = threadIdx.x, lane = tid & 63, wave = __builtin_amdgcn_readfirstlane(tid >> 6);
  const int r = lane >> 4, c = lane & 15;
  const int b = wave & 1, wv = wave >> 1;
  constexpr int HKV = 32 / G, HW = WAVES / 2;
  gptr xp = (gptr)(br ? a.x[1] : a.x[0]);
  gptr wqp = (gptr)(br ? a.wq[1] : a.wq[0]);
  gptr wop = (gptr)(br ? a.wo[1] : a.wo[0]);
  const rsrc_t kp = mkrsrc((gptr)(br ? a.k[1] : a.k[0]) + (size_t)(b * HKV + kvh) * (SK * DH * 2), SK * DH * 2);
  const rsrc_t vp = mkrsrc((gptr)(br ? a.v[1] : a.v[0]) + (size_t)(b * HKV + kvh) * (SK * DH * 2), SK * DH * 2);
  const unsigned loff = lane * 16;
  int t = t0 + wv;

  u32x4 q[G];
  Tile ta, tb;
  TS(0);
  {
    unsigned xr[9];
#pragma unroll
    for (int i = 0; i < 9; ++i)
      xr[i] = *(const __attribute__((address_space(1))) unsigned*)(xp + wave * 144 + i * 4 + (unsigned)((c & 1) * (DM * 2) + r * 36));
    u32x4 w[G][18];
#pragma unroll
    for (int g = 0; g < G; ++g)
#pragma unroll
      for (int i = 0; i < 18; ++i)
        w[g][i] = ld16(wqp + (size_t)(wave * 72 + i) * (NQ * 2) + (kvh * G + g) * (DH * 2), (unsigned)(r * 18 * NQ * 2 + c * 16));
    __builtin_amdgcn_sched_barrier(0);
    __builtin_amdgcn_s_barrier();
    load_tile(ta, kp, vp, t, loff);
    if (PF2 == 2 || (PF2 == 1 && G == 1)) load_tile(tb, kp, vp, t + HW, loff);
    __builtin_amdgcn_sched_barrier(0);
    float* qp = reinterpret_cast<float*>(lds + LDS_Q);
#pragma unroll
    for (int g = 0; g < G; ++g) {
      f32x4 d[8];
#pragma unroll
      for (int e = 0; e < 8; ++e) d[e] = f32x4{0.f, 0.f, 0.f, 0.f};
      mm8(d, u32x4{xr[0], xr[1], xr[2], xr[3]}, &w[g][0]);
      __builtin_amdgcn_sched_barrier(0);
      mm8(d, u32x4{xr[4], xr[5], xr[6], xr[7]}, &w[g][8]);
      __builtin_amdgcn_sched_barrier(0);
#pragma unroll
      for (int e = 0; e < 8; ++e) d[e] = mfma16(u32x2{xr[8], 0u}, u32x2{pairk(w[g][16][e >> 1], w[g][17][e >> 1], e & 1), 0u}, d[e]);
      f32x4 o4;
#pragma unroll
      for (int e = 0; e < 4; ++e) {
        const float lo = (r & 2) ? d[e][1] : d[e][0], hi = (r & 2) ? d[e + 4][1] : d[e + 4][0];
        o4[e] = (r & 1) ? hi : lo;
      }
      *reinterpret_cast<f32x4*>(qp + ((wave * G + g) * 2 + (r >> 1)) * DH + c * 8 + (r & 1) * 4) = o4;
    }
    LBAR();
#pragma unroll
    for (int g = 0; g < G; ++g) {
      f32x4 lo = {0.f, 0.f, 0.f, 0.f}, hi = {0.f, 0.f, 0.f, 0.f};
#pragma unroll
      for (int w2 = 0; w2 < WAVES; ++w2) {
        lo += *reinterpret_cast<const f32x4*>(qp + ((w2 * G + g) * 2 + b) * DH + c * 8);
        hi += *reinterpret_cast<const f32x4*>(qp + ((w2 * G + g) * 2 + b) * DH + c * 8 + 4);
      }
      q[g] = u32x4{cvtpk(lo[0], lo[1]), cvtpk(lo[2], lo[3]), cvtpk(hi[0], hi[1]), cvtpk(hi[2], hi[3])};
    }
  }
  __builtin_amdgcn_sched_barrier(0);
  u32x4 wm[16];
  u32x2 wsd[G][4];
  {
    gptr wh = wop + (size_t)(kvh * G * DH) * (DM * 2);
    const unsigned mo = (unsigned)((b * 64 + r * 8) * (DM * 2) + wv * 256 + c * 16);
    const unsigned so = (unsigned)((wave * 16 + r * 4) * (DM * 2) + 1024 + c * 8);
#pragma unroll
    for (int i = 0; i < 16; ++i) wm[i] = ld16(wh + ((i >> 3) * 32 + (i & 7)) * (DM * 2), mo);
#pragma unroll
    for (int g = 0; g < G; ++g)
#pragma unroll
      for (int i = 0; i < 4; ++i)
        wsd[g][i] = __builtin_nontemporal_load((const __attribute__((address_space(1))) u32x2*)(wh + (g * DH + i) * (DM * 2) + so));
    if (G == 2) {
      const unsigned lb = (unsigned)(size_t)(__attribute__((address_space(3))) char*)lds + wave * 16384;
#pragma unroll
      for (int i = 0; i < 16; ++i)
        asm volatile("s_mov_b32 m0, %0\n\ts_nop 0\n\tglobal_load_lds_dwordx4 %1, %2 nt"
                     :: "s"(lb + i * 1024), "v"(mo), "s"(wh + (DH + (i >> 3) * 32 + (i & 7)) * (DM * 2)) : "m0", "memory");
    }
  }
  __builtin_amdgcn_sched_barrier(0);

  TS(1);
  float m[G], l[G];
  f32x2 acc[G][4];
#pragma unroll
  for (int g = 0; g < G; ++g) { m[g] = -INFINITY; l[g] = 0.f;
#pragma unroll
    for (int e = 0; e < 4; ++e) acc[g][e] = f32x2{0.f, 0.f}; }
  if (!(PF2 == 2 || (PF2 == 1 && G == 1))) load_tile(tb, kp, vp, t + HW, loff);
  t += 2 * HW;
#define SB() __builtin_amdgcn_sched_barrier(0)
  for (; t + HW < t1; t += 2 * HW) {
    compute_tile<G>(ta, q, m, l, acc); SB();
    load_tile(ta, kp, vp, t, loff); SB();
    compute_tile<G>(tb, q, m, l, acc); SB();
    load_tile(tb, kp, vp, t + HW, loff); SB();
  }
  compute_tile<G>(ta, q, m, l, acc); SB();
  const bool more = t < t1;
  if (more) load_tile(ta, kp, vp, t, loff);
  SB();
  compute_tile<G>(tb, q, m, l, acc); SB();
  if (more) compute_tile<G>(ta, q, m, l, acc);
  TS(2);
  u32x4 wm2[G == 2 ? 16 : 1];
  if (G == 2) {
    asm volatile("s_waitcnt vmcnt(0)" ::: "memory");
#pragma unroll
    for (int i = 0; i < 16; ++i) wm2[i] = *reinterpret_cast<const u32x4*>(lds + wave * 16384 + i * 1024 + lane * 16);
  }

  f32x2* mlw = reinterpret_cast<f32x2*>(lds + LDS_ML);
#pragma unroll
  for (int g = 0; g < G; ++g) {
    const float M = xrow_max(m[g]);
    const float sc = __builtin_amdgcn_exp2f(m[g] - M);
    const float L = xrow_sum(l[g] * sc);
    float h[4];
#pragma unroll
    for (int e = 0; e < 4; ++e) {
      const float lo = acc[g][e >> 1][e & 1] * sc, hi = acc[g][2 + (e >> 1)][e & 1] * sc;
      auto s = __builtin_amdgcn_permlane32_swap(__float_as_uint(lo), __float_as_uint(hi), false, false);
      h[e] = __uint_as_float(s[0]) + __uint_as_float(s[1]);
    }
    f32x2 o2;
#pragma unroll
    for (int e = 0; e < 2; ++e) {
      auto s = __builtin_amdgcn_permlane16_swap(__float_as_uint(h[e]), __float_as_uint(h[e + 2]), false, false);
      o2[e] = __uint_as_float(s[0]) + __uint_as_float(s[1]);
    }
    const unsigned ph = cvtpk(o2[0], o2[1]);
    const unsigned pl = cvtpk(o2[0] - bflo(ph), o2[1] - bfhi(ph));
    char* rp = lds + LDS_REC + (g * 16 + wv * 4 + b) * RECS + c * 16 + r * 4;
    *reinterpret_cast<unsigned*>(rp) = ph;
    *reinterpret_cast<unsigned*>(rp + 2 * RECS) = pl;
    if (lane == 0) mlw[wave * G + g] = f32x2{M, L};
  }
  LBAR();
  TS(3);
  float* yl = reinterpret_cast<float*>(lds + LDS_Q);
  f32x4 ym[G];
  f32x2 mlout[G];
#pragma unroll
  for (int g = 0; g < G; ++g) {
    const char* rp = lds + LDS_REC + (g * 16 + c) * RECS;
    const u32x4 a0 = *reinterpret_cast<const u32x4*>(rp + b * 128 + r * 16);
    const u32x4 a1 = *reinterpret_cast<const u32x4*>(rp + b * 128 + 64 + r * 16);
    const u32x2 a2 = *reinterpret_cast<const u32x2*>(rp + wave * 32 + r * 8);
    const f32x2 ml0 = mlw[(r * 2) * G + g], ml1 = mlw[(r * 2 + 1) * G + g];
    f32x4 d[8], ds[4];
#pragma unroll
    for (int e = 0; e < 8; ++e) d[e] = f32x4{0.f, 0.f, 0.f, 0.f};
    mm8(d, a0, g ? &wm2[0] : &wm[0]);
    mm8(d, a1, g ? &wm2[G == 2 ? 8 : 0] : &wm[8]);
#pragma unroll
    for (int e = 0; e < 4; ++e)
      ds[e] = mfma16(a2, u32x2{pairk(wsd[g][0][e >> 1], wsd[g][1][e >> 1], e & 1), pairk(wsd[g][2][e >> 1], wsd[g][3][e >> 1], e & 1)},
                     f32x4{0.f, 0.f, 0.f, 0.f});
    const float M0 = xrow_max(ml0[0]), M1 = xrow_max(ml1[0]);
    const float w0 = __builtin_amdgcn_exp2f(ml0[0] - M0), w1 = __builtin_amdgcn_exp2f(ml1[0] - M1);
    const float L0 = xrow_sum(w0 * ml0[1]), L1 = xrow_sum(w1 * ml1[1]);
    mlout[g] = (r & 1) ? f32x2{M1, L1} : f32x2{M0, L0};
    float h[8], hs[4];
#pragma unroll
    for (int e = 0; e < 8; ++e) {
      auto s = __builtin_amdgcn_permlane32_swap(__float_as_uint(w0 * (d[e][0] + d[e][2])), __float_as_uint(w1 * (d[e][1] + d[e][3])), false, false);
      h[e] = __uint_as_float(s[0]) + __uint_as_float(s[1]);
    }
#pragma unroll
    for (int e = 0; e < 4; ++e) {
      auto s = __builtin_amdgcn_permlane32_swap(__float_as_uint(w0 * (ds[e][0] + ds[e][2])), __float_as_uint(w1 * (ds[e][1] + ds[e][3])), false, false);
      hs[e] = __uint_as_float(s[0]) + __uint_as_float(s[1]);
    }
#pragma unroll
    for (int e = 0; e < 4; ++e) {
      auto s = __builtin_amdgcn_permlane16_swap(__float_as_uint(h[e]), __float_as_uint(h[e + 4]), false, false);
      ym[g][e] = __uint_as_float(s[0]) + __uint_as_float(s[1]);
    }
    f32x2 ys;
#pragma unroll
    for (int e = 0; e < 2; ++e) {
      auto s = __builtin_amdgcn_permlane16_swap(__float_as_uint(hs[e]), __float_as_uint(hs[e + 2]), false, false);
      ys[e] = __uint_as_float(s[0]) + __uint_as_float(s[1]);
    }
    if (b) *reinterpret_cast<f32x4*>(yl + ((g * 4 + wv) * 64 + lane) * 4) = ym[g];
    *reinterpret_cast<f32x2*>(yl + 2048 + ((g * 8 + wave) * 64 + lane) * 2) = ys;
  }
  LBAR();
  float* yws = a.ws + WS_Y / 4 + ((size_t)((br * 2 + (r >> 1)) * 32 + kvh * G) * SMAX + j) * DM;
  if (!b) {
#pragma unroll
    for (int g = 0; g < G; ++g)
      *reinterpret_cast<f32x4*>(yws + g * (SMAX * DM) + wv * 128 + c * 8 + (r & 1) * 4) =
          ym[g] + *reinterpret_cast<const f32x4*>(yl + ((g * 4 + wv) * 64 + lane) * 4);
  } else if (wv < G) {
    f32x2 s = {0.f, 0.f};
#pragma unroll
    for (int w2 = 0; w2 < WAVES; ++w2) s += *reinterpret_cast<const f32x2*>(yl + 2048 + ((wv * 8 + w2) * 64 + lane) * 2);
    *reinterpret_cast<f32x2*>(yws + wv * (SMAX * DM) + 512 + c * 4 + (r & 1) * 2) = s;
  } else if (wv == 3 && (lane & 47) == 0) {
    f32x2* ml = reinterpret_cast<f32x2*>(a.ws + WS_ML / 4);
#pragma unroll
    for (int g = 0; g < G; ++g) ml[((br * 2 + (r & 1)) * 32 + kvh * G + g) * SMAX + j] = mlout[g];
  }
  TS(4);
}

__global__ void __launch_bounds__(NTHR, 2) attn_main(Args a) {
  __shared__ __attribute__((aligned(16))) char lds[LDS_BYTES];
  const int xcd = blockIdx.x & 7, slot = blockIdx.x >> 3;
  int isa, h, j;
  isa = slot < A_PER_X;
  if (isa) { h = ((xcd + AOFF) & 7) * 2 + slot / NWA; j = slot % NWA; }
  else { h = xcd * 4 + (slot - A_PER_X) / NWB; j = (slot - A_PER_X) % NWB; }
  const bool sh = false;
  if (isa) body<2>(a, 0, h, j, (TILES * j) / NWA, (TILES * (j + 1)) / NWA, lds);
  else {
    const int t0 = sh ? ((TILES - HT) * j) / 4 : (TILES * j) / NWB;
    const int t1 = sh ? (j == 4 ? TILES : ((TILES - HT) * (j + 1)) / 4) : (TILES * (j + 1)) / NWB;
    body<1>(a, 1, h, j, t0, t1, lds);
  }
}
#define IS_B(i) (((i) >> 3) >= A_PER_X)

__global__ void __launch_bounds__(256) attn_final(const float* ws, __hip_bfloat16* out) {
  __shared__ __attribute__((aligned(16))) float red[64 * 16];
  const int chunk = blockIdx.x % 36, bb = (blockIdx.x / 36) & 1, br = blockIdx.x / 72;
  const int tid = threadIdx.x, q4 = tid & 3, vi = tid >> 2;
  const int h = vi >> 1, half = vi & 1;
  const int S = br ? NWB : NWA;
  const f32x2* ml = reinterpret_cast<const f32x2*>(ws + WS_ML / 4) + ((br * 2 + bb) * 32 + h) * SMAX;
  const float* y = ws + WS_Y / 4 + (size_t)(((br * 2 + bb) * 32 + h) * SMAX) * DM + chunk * 16 + q4 * 4;
  f32x4 yv[3];
  f32x2 mv[6];
#pragma unroll
  for (int i = 0; i < 3; ++i) { const int s = min(half * 3 + i, S - 1); yv[i] = *reinterpret_cast<const f32x4*>(y + s * DM); }
#pragma unroll
  for (int s = 0; s < 6; ++s) mv[s] = ml[min(s, S - 1)];
  if (S == 5) mv[5] = f32x2{-INFINITY, 0.f};
  float M = mv[0][0];
#pragma unroll
  for (int s = 1; s < 6; ++s) M = fmaxf(M, mv[s][0]);
  float L = 0.f, wt[6];
#pragma unroll
  for (int s = 0; s < 6; ++s) { wt[s] = __builtin_amdgcn_exp2f(mv[s][0] - M); L = fmaf(wt[s], mv[s][1], L); }
  const float inv = 1.f / L;
  const float w0 = half ? wt[3] : wt[0], w1 = half ? wt[4] : wt[1], w2 = half ? wt[5] : wt[2];
  const f32x4 sacc = (yv[0] * w0 + yv[1] * w1 + yv[2] * w2) * inv;
  *reinterpret_cast<f32x4*>(red + vi * 16 + q4 * 4) = sacc;
  __syncthreads();
  if (tid < 64) {
    const int col = tid & 15, part = tid >> 4;
    float s = 0.f;
#pragma unroll
    for (int i = 0; i < 16; ++i) s += red[(part * 16 + i) * 16 + col];
    s = xrow_sum(s);
    if (tid < 16) out[bb * (2 * DM) + br * DM + chunk * 16 + col] = __float2bfloat16(s);
  }
}

extern "C" void kernel_launch(void* const* d_in, const int* in_sizes, int n_in,
                              void* d_out, int out_size, void* d_ws, size_t ws_size,
                              hipStream_t stream) {
  Args a;
  for (int i = 0; i < 2; ++i) {
    a.x[i] = (const unsigned short*)d_in[5 * i + 0];
    a.k[i] = (const unsigned short*)d_in[5 * i + 1];
    a.v[i] = (const unsigned short*)d_in[5 * i + 2];
    a.wq[i] = (const unsigned short*)d_in[5 * i + 3];
    a.wo[i] = (const unsigned short*)d_in[5 * i + 4];
  }
  a.ws = (float*)d_ws;
  hipLaunchKernelGGL(attn_main, dim3(GRID1), dim3(NTHR), 0, stream, a);
  hipLaunchKernelGGL(attn_final, dim3(144), dim3(256), 0, stream, (const float*)d_ws, (__hip_bfloat16*)d_out);
}
```

```cpp
#define CPF 1
#include <hip/hip_runtime.h>
#include <hip/hip_bf16.h>
#include <stdint.h>

typedef __attribute__((ext_vector_type(4))) unsigned u32x4;
typedef __attribute__((ext_vector_type(2))) unsigned u32x2;
typedef __attribute__((ext_vector_type(4))) float f32x4;
typedef __attribute__((ext_vector_type(2))) float f32x2;
typedef __attribute__((ext_vector_type(2))) __bf16 bf16x2;
typedef __attribute__((ext_vector_type(8))) __bf16 bf16x8;
typedef __attribute__((ext_vector_type(4))) short s16x4;

#define NWA 6
#define NWB 5
#define KVAUX 2
#define QBAR 1
#define WNT 1
#define PF2 1
#define BIAS 0
#define HT 102
#define LOCK 0
#define AOFF 1

#define TS(i) do {} while (0)
constexpr int DM = 576, DH = 128, SK = 8192, NQ = 4096;
constexpr int WAVES = 8, NTHR = WAVES * 64;
constexpr int TILES = SK / 16;
constexpr int SMAX = 8;
constexpr int A_PER_X = 2 * NWA, B_PER_X = 4 * NWB, WG_PER_X = A_PER_X + B_PER_X;
constexpr int GRID1 = 8 * WG_PER_X;
constexpr float C2 = 0.088388347648318440f * 1.4426950408889634f;

constexpr size_t WS_ML = 0, WS_Y = 65536;
constexpr int RECS = 272;
constexpr int LDS_Q = 131072, LDS_REC = LDS_Q + 16384, LDS_ML = LDS_REC + 2 * 16 * RECS, LDS_BYTES = LDS_ML + 128;

struct Args {
  const unsigned short* x[2];
  const unsigned short* k[2];
  const unsigned short* v[2];
  const unsigned short* wq[2];
  const unsigned short* wo[2];
  float* ws;
};

__device__ __forceinline__ float dot2(unsigned a, unsigned b, float acc) {
  return __builtin_amdgcn_fdot2_f32_bf16(__builtin_bit_cast(bf16x2, a), __builtin_bit_cast(bf16x2, b), acc, false);
}
__device__ __forceinline__ unsigned cvtpk(float lo, float hi) {
  unsigned r; asm("v_cvt_pk_bf16_f32 %0, %1, %2" : "=v"(r) : "v"(lo), "v"(hi)); return r;
}
template <int CTRL> __device__ __forceinline__ float dpp(float x) {
  return __builtin_bit_cast(float, __builtin_amdgcn_mov_dpp(__builtin_bit_cast(int, x), CTRL, 0xf, 0xf, true));
}
constexpr int XOR1 = 0xB1, XOR2 = 0x4E, XOR7 = 0x141, XOR8 = 0x128;
__device__ __forceinline__ float row_sum16(float x) {
  x += dpp<XOR8>(x); x += dpp<XOR7>(x); x += dpp<XOR2>(x); x += dpp<XOR1>(x); return x;
}
__device__ __forceinline__ float xrow_max(float x) {
  auto s = __builtin_amdgcn_permlane16_swap(__float_as_uint(x), __float_as_uint(x), false, false);
  x = fmaxf(__uint_as_float(s[0]), __uint_as_float(s[1]));
  auto t = __builtin_amdgcn_permlane32_swap(__float_as_uint(x), __float_as_uint(x), false, false);
  return fmaxf(__uint_as_float(t[0]), __uint_as_float(t[1]));
}
__device__ __forceinline__ float xrow_sum(float x) {
  auto s = __builtin_amdgcn_permlane16_swap(__float_as_uint(x), __float_as_uint(x), false, false);
  x = __uint_as_float(s[0]) + __uint_as_float(s[1]);
  auto t = __builtin_amdgcn_permlane32_swap(__float_as_uint(x), __float_as_uint(x), false, false);
  return __uint_as_float(t[0]) + __uint_as_float(t[1]);
}
__device__ __forceinline__ float bflo(unsigned u) { return __uint_as_float(u << 16); }
__device__ __forceinline__ float bfhi(unsigned u) { return __uint_as_float(u & 0xffff0000u); }
typedef const __attribute__((address_space(1))) char* gptr;
typedef const __attribute__((address_space(1))) u32x4* gvec;
typedef __amdgpu_buffer_rsrc_t rsrc_t;
__device__ __forceinline__ rsrc_t mkrsrc(gptr p, unsigned bytes) {
  return __builtin_amdgcn_make_buffer_rsrc((void*)(const char*)p, (short)0, (int)bytes, 0x00020000);
}
__device__ __forceinline__ u32x4 ld16(gptr base, unsigned off) {
  return __builtin_nontemporal_load((gvec)(base + off));
}

struct Tile { u32x4 k[4]; u32x4 v[4]; };

__device__ __forceinline__ void load_tile(Tile& t, rsrc_t kr, rsrc_t vr, int tile, unsigned loff) {
#pragma unroll
  for (int i = 0; i < 4; ++i) t.k[i] = __builtin_amdgcn_raw_buffer_load_b128(kr, loff + i * 1024, tile * 4096, KVAUX);
#pragma unroll
  for (int i = 0; i < 4; ++i) t.v[i] = __builtin_amdgcn_raw_buffer_load_b128(vr, loff + i * 1024, tile * 4096, KVAUX);
}

template <int G>
__device__ __forceinline__ void compute_tile(const Tile& t, const u32x4 (&q)[G], float (&m)[G], float (&l)[G], f32x2 (&acc)[G][4]) {
  float p[G][4];
#pragma unroll
  for (int g = 0; g < G; ++g) {
    float s[4];
#pragma unroll
    for (int i = 0; i < 4; ++i) {
      float d = dot2(t.k[i][0], q[g][0], 0.f);
      d = dot2(t.k[i][1], q[g][1], d);
      d = dot2(t.k[i][2], q[g][2], d);
      d = dot2(t.k[i][3], q[g][3], d);
      s[i] = d;
    }
#pragma unroll
    for (int i = 0; i < 4; ++i) s[i] = row_sum16(s[i]) * C2;
    const float tm = fmaxf(fmaxf(s[0], s[1]), fmaxf(s[2], s[3]));
    if (__builtin_amdgcn_ballot_w64(tm > m[g]) != 0ull) {
      const float mn = fmaxf(m[g], tm);
      const float al = __builtin_amdgcn_exp2f(m[g] - mn);
      m[g] = mn; l[g] *= al;
#pragma unroll
      for (int j = 0; j < 4; ++j) acc[g][j] *= al;
    }
#pragma unroll
    for (int i = 0; i < 4; ++i) p[g][i] = __builtin_amdgcn_exp2f(s[i] - m[g]);
    l[g] += (p[g][0] + p[g][1]) + (p[g][2] + p[g][3]);
  }
#pragma unroll
  for (int i = 0; i < 4; ++i) {
#pragma unroll
    for (int j = 0; j < 4; ++j) {
      const f32x2 vf = {bflo(t.v[i][j]), bfhi(t.v[i][j])};
#pragma unroll
      for (int g = 0; g < G; ++g) {
        const f32x2 pp = {p[g][i], p[g][i]};
        acc[g][j] = __builtin_elementwise_fma(pp, vf, acc[g][j]);
      }
    }
  }
}

#define LBAR() asm volatile("s_waitcnt lgkmcnt(0)\n\ts_barrier" ::: "memory")
__device__ __forceinline__ f32x4 mfma32(u32x4 a, u32x4 b, f32x4 c) {
  return __builtin_amdgcn_mfma_f32_16x16x32_bf16(__builtin_bit_cast(bf16x8, a), __builtin_bit_cast(bf16x8, b), c, 0, 0, 0);
}
__device__ __forceinline__ f32x4 mfma16(u32x2 a, u32x2 b, f32x4 c) {
  return __builtin_amdgcn_mfma_f32_16x16x16bf16_1k(__builtin_bit_cast(s16x4, a), __builtin_bit_cast(s16x4, b), c, 0, 0, 0);
}
__device__ __forceinline__ unsigned pairk(unsigned r0, unsigned r1, int odd) {
  return __builtin_amdgcn_perm(r1, r0, odd ? 0x07060302u : 0x05040100u);
}
__device__ __forceinline__ void mm8(f32x4 (&d)[8], const u32x4 a, const u32x4* w) {
#pragma unroll
  for (int j = 0; j < 8; ++j) {
    const u32x4 bv = {pairk(w[0][j >> 1], w[1][j >> 1], j & 1), pairk(w[2][j >> 1], w[3][j >> 1], j & 1),
                      pairk(w[4][j >> 1], w[5][j >> 1], j & 1), pairk(w[6][j >> 1], w[7][j >> 1], j & 1)};
    d[j] = mfma32(a, bv, d[j]);
  }
}

template <int G>
__device__ __forceinline__ void body(const Args& a, const int br, const int kvh, const int j, const int t0, const int t1, char* lds) {
  const int tid = threadIdx.x, lane = tid & 63, wave = __builtin_amdgcn_readfirstlane(tid >> 6);
  const int r = lane >> 4, c = lane & 15;
  const int b = wave & 1, wv = wave >> 1;
  constexpr int HKV = 32 / G, HW = WAVES / 2;
  gptr xp = (gptr)(br ? a.x[1] : a.x[0]);
  gptr wqp = (gptr)(br ? a.wq[1] : a.wq[0]);
  gptr wop = (gptr)(br ? a.wo[1] : a.wo[0]);
  const rsrc_t kp = mkrsrc((gptr)(br ? a.k[1] : a.k[0]) + (size_t)(b * HKV + kvh) * (SK * DH * 2), SK * DH * 2);
  const rsrc_t vp = mkrsrc((gptr)(br ? a.v[1] : a.v[0]) + (size_t)(b * HKV + kvh) * (SK * DH * 2), SK * DH * 2);
  const unsigned loff = lane * 16;
  int t = t0 + wv;

  u32x4 q[G];
  Tile ta, tb;
  TS(0);
  {
    unsigned xr[9];
#pragma unroll
    for (int i = 0; i < 9; ++i)
      xr[i] = *(const __attribute__((address_space(1))) unsigned*)(xp + wave * 144 + i * 4 + (unsigned)((c & 1) * (DM * 2) + r * 36));
    u32x4 w[G][18];
#pragma unroll
    for (int g = 0; g < G; ++g)
#pragma unroll
      for (int i = 0; i < 18; ++i)
        w[g][i] = ld16(wqp + (size_t)(wave * 72 + i) * (NQ * 2) + (kvh * G + g) * (DH * 2), (unsigned)(r * 18 * NQ * 2 + c * 16));
    __builtin_amdgcn_sched_barrier(0);
    __builtin_amdgcn_s_barrier();
    load_tile(ta, kp, vp, t, loff);
    if (PF2 == 2 || (PF2 == 1 && G == 1)) load_tile(tb, kp, vp, t + HW, loff);
    __builtin_amdgcn_sched_barrier(0);
    float* qp = reinterpret_cast<float*>(lds + LDS_Q);
#pragma unroll
    for (int g = 0; g < G; ++g) {
      f32x4 d[8];
#pragma unroll
      for (int e = 0; e < 8; ++e) d[e] = f32x4{0.f, 0.f, 0.f, 0.f};
      mm8(d, u32x4{xr[0], xr[1], xr[2], xr[3]}, &w[g][0]);
      __builtin_amdgcn_sched_barrier(0);
      mm8(d, u32x4{xr[4], xr[5], xr[6], xr[7]}, &w[g][8]);
      __builtin_amdgcn_sched_barrier(0);
#pragma unroll
      for (int e = 0; e < 8; ++e) d[e] = mfma16(u32x2{xr[8], 0u}, u32x2{pairk(w[g][16][e >> 1], w[g][17][e >> 1], e & 1), 0u}, d[e]);
      f32x4 o4;
#pragma unroll
      for (int e = 0; e < 4; ++e) {
        const float lo = (r & 2) ? d[e][1] : d[e][0], hi = (r & 2) ? d[e + 4][1] : d[e + 4][0];
        o4[e] = (r & 1) ? hi : lo;
      }
      *reinterpret_cast<f32x4*>(qp + ((wave * G + g) * 2 + (r >> 1)) * DH + c * 8 + (r & 1) * 4) = o4;
    }
    LBAR();
#pragma unroll
    for (int g = 0; g < G; ++g) {
      f32x4 lo = {0.f, 0.f, 0.f, 0.f}, hi = {0.f, 0.f, 0.f, 0.f};
#pragma unroll
      for (int w2 = 0; w2 < WAVES; ++w2) {
        lo += *reinterpret_cast<const f32x4*>(qp + ((w2 * G + g) * 2 + b) * DH + c * 8);
        hi += *reinterpret_cast<const f32x4*>(qp + ((w2 * G + g) * 2 + b) * DH + c * 8 + 4);
      }
      q[g] = u32x4{cvtpk(lo[0], lo[1]), cvtpk(lo[2], lo[3]), cvtpk(hi[0], hi[1]), cvtpk(hi[2], hi[3])};
    }
  }
  __builtin_amdgcn_sched_barrier(0);
  u32x4 wm[16];
  u32x2 wsd[G][4];
  {
    gptr wh = wop + (size_t)(kvh * G * DH) * (DM * 2);
    const unsigned mo = (unsigned)((b * 64 + r * 8) * (DM * 2) + wv * 256 + c * 16);
    const unsigned so = (unsigned)((wave * 16 + r * 4) * (DM * 2) + 1024 + c * 8);
#pragma unroll
    for (int i = 0; i < 16; ++i) wm[i] = ld16(wh + ((i >> 3) * 32 + (i & 7)) * (DM * 2), mo);
#pragma unroll
    for (int g = 0; g < G; ++g)
#pragma unroll
      for (int i = 0; i < 4; ++i)
        wsd[g][i] = __builtin_nontemporal_load((const __attribute__((address_space(1))) u32x2*)(wh + (g * DH + i) * (DM * 2) + so));
    if (G == 2) {
      const unsigned lb = (unsigned)(size_t)(__attribute__((address_space(3))) char*)lds + wave * 16384;
#pragma unroll
      for (int i = 0; i < 16; ++i)
        asm volatile("s_mov_b32 m0, %0\n\ts_nop 0\n\tglobal_load_lds_dwordx4 %1, %2 nt"
                     :: "s"(lb + i * 1024), "v"(mo), "s"(wh + (DH + (i >> 3) * 32 + (i & 7)) * (DM * 2)) : "m0", "memory");
    }
  }
  __builtin_amdgcn_sched_barrier(0);

  TS(1);
  float m[G], l[G];
  f32x2 acc[G][4];
#pragma unroll
  for (int g = 0; g < G; ++g) { m[g] = -INFINITY; l[g] = 0.f;
#pragma unroll
    for (int e = 0; e < 4; ++e) acc[g][e] = f32x2{0.f, 0.f}; }
  if (!(PF2 == 2 || (PF2 == 1 && G == 1))) load_tile(tb, kp, vp, t + HW, loff);
  t += 2 * HW;
#define SB() __builtin_amdgcn_sched_barrier(0)
  for (; t + HW < t1; t += 2 * HW) {
    compute_tile<G>(ta, q, m, l, acc); SB();
    load_tile(ta, kp, vp, t, loff); SB();
    compute_tile<G>(tb, q, m, l, acc); SB();
    load_tile(tb, kp, vp, t + HW, loff); SB();
  }
  compute_tile<G>(ta, q, m, l, acc); SB();
  const bool more = t < t1;
  if (more) load_tile(ta, kp, vp, t, loff);
  SB();
  compute_tile<G>(tb, q, m, l, acc); SB();
  if (more) compute_tile<G>(ta, q, m, l, acc);
  TS(2);
  u32x4 wm2[G == 2 ? 16 : 1];
  if (G == 2) {
    asm volatile("s_waitcnt vmcnt(0)" ::: "memory");
#pragma unroll
    for (int i = 0; i < 16; ++i) wm2[i] = *reinterpret_cast<const u32x4*>(lds + wave * 16384 + i * 1024 + lane * 16);
  }

  f32x2* mlw = reinterpret_cast<f32x2*>(lds + LDS_ML);
#pragma unroll
  for (int g = 0; g < G; ++g) {
    const float M = xrow_max(m[g]);
    const float sc = __builtin_amdgcn_exp2f(m[g] - M);
    const float L = xrow_sum(l[g] * sc);
    float h[4];
#pragma unroll
    for (int e = 0; e < 4; ++e) {
      const float lo = acc[g][e >> 1][e & 1] * sc, hi = acc[g][2 + (e >> 1)][e & 1] * sc;
      auto s = __builtin_amdgcn_permlane32_swap(__float_as_uint(lo), __float_as_uint(hi), false, false);
      h[e] = __uint_as_float(s[0]) + __uint_as_float(s[1]);
    }
    f32x2 o2;
#pragma unroll
    for (int e = 0; e < 2; ++e) {
      auto s = __builtin_amdgcn_permlane16_swap(__float_as_uint(h[e]), __float_as_uint(h[e + 2]), false, false);
      o2[e] = __uint_as_float(s[0]) + __uint_as_float(s[1]);
    }
    const unsigned ph = cvtpk(o2[0], o2[1]);
    const unsigned pl = cvtpk(o2[0] - bflo(ph), o2[1] - bfhi(ph));
    char* rp = lds + LDS_REC + (g * 16 + wv * 4 + b) * RECS + c * 16 + r * 4;
    *reinterpret_cast<unsigned*>(rp) = ph;
    *reinterpret_cast<unsigned*>(rp + 2 * RECS) = pl;
    if (lane == 0) mlw[wave * G + g] = f32x2{M, L};
  }
  LBAR();
  TS(3);
  float* yl = reinterpret_cast<float*>(lds + LDS_Q);
  f32x4 ym[G];
  f32x2 mlout[G];
#pragma unroll
  for (int g = 0; g < G; ++g) {
    const char* rp = lds + LDS_REC + (g * 16 + c) * RECS;
    const u32x4 a0 = *reinterpret_cast<const u32x4*>(rp + b * 128 + r * 16);
    const u32x4 a1 = *reinterpret_cast<const u32x4*>(rp + b * 128 + 64 + r * 16);
    const u32x2 a2 = *reinterpret_cast<const u32x2*>(rp + wave * 32 + r * 8);
    const f32x2 ml0 = mlw[(r * 2) * G + g], ml1 = mlw[(r * 2 + 1) * G + g];
    f32x4 d[8], ds[4];
#pragma unroll
    for (int e = 0; e < 8; ++e) d[e] = f32x4{0.f, 0.f, 0.f, 0.f};
    mm8(d, a0, g ? &wm2[0] : &wm[0]);
    mm8(d, a1, g ? &wm2[G == 2 ? 8 : 0] : &wm[8]);
#pragma unroll
    for (int e = 0; e < 4; ++e)
      ds[e] = mfma16(a2, u32x2{pairk(wsd[g][0][e >> 1], wsd[g][1][e >> 1], e & 1), pairk(wsd[g][2][e >> 1], wsd[g][3][e >> 1], e & 1)},
                     f32x4{0.f, 0.f, 0.f, 0.f});
    const float M0 = xrow_max(ml0[0]), M1 = xrow_max(ml1[0]);
    const float w0 = __builtin_amdgcn_exp2f(ml0[0] - M0), w1 = __builtin_amdgcn_exp2f(ml1[0] - M1);
    const float L0 = xrow_sum(w0 * ml0[1]), L1 = xrow_sum(w1 * ml1[1]);
    mlout[g] = (r & 1) ? f32x2{M1, L1} : f32x2{M0, L0};
    float h[8], hs[4];
#pragma unroll
    for (int e = 0; e < 8; ++e) {
      auto s = __builtin_amdgcn_permlane32_swap(__float_as_uint(w0 * (d[e][0] + d[e][2])), __float_as_uint(w1 * (d[e][1] + d[e][3])), false, false);
      h[e] = __uint_as_float(s[0]) + __uint_as_float(s[1]);
    }
#pragma unroll
    for (int e = 0; e < 4; ++e) {
      auto s = __builtin_amdgcn_permlane32_swap(__float_as_uint(w0 * (ds[e][0] + ds[e][2])), __float_as_uint(w1 * (ds[e][1] + ds[e][3])), false, false);
      hs[e] = __uint_as_float(s[0]) + __uint_as_float(s[1]);
    }
#pragma unroll
    for (int e = 0; e < 4; ++e) {
      auto s = __builtin_amdgcn_permlane16_swap(__float_as_uint(h[e]), __float_as_uint(h[e + 4]), false, false);
      ym[g][e] = __uint_as_float(s[0]) + __uint_as_float(s[1]);
    }
    f32x2 ys;
#pragma unroll
    for (int e = 0; e < 2; ++e) {
      auto s = __builtin_amdgcn_permlane16_swap(__float_as_uint(hs[e]), __float_as_uint(hs[e + 2]), false, false);
      ys[e] = __uint_as_float(s[0]) + __uint_as_float(s[1]);
    }
    if (b) *reinterpret_cast<f32x4*>(yl + ((g * 4 + wv) * 64 + lane) * 4) = ym[g];
    *reinterpret_cast<f32x2*>(yl + 2048 + ((g * 8 + wave) * 64 + lane) * 2) = ys;
  }
  LBAR();
  float* yws = a.ws + WS_Y / 4 + ((size_t)((br * 2 + (r >> 1)) * 32 + kvh * G) * SMAX + j) * DM;
  if (!b) {
#pragma unroll
    for (int g = 0; g < G; ++g)
      *reinterpret_cast<f32x4*>(yws + g * (SMAX * DM) + wv * 128 + c * 8 + (r & 1) * 4) =
          ym[g] + *reinterpret_cast<const f32x4*>(yl + ((g * 4 + wv) * 64 + lane) * 4);
  } else if (wv < G) {
    f32x2 s = {0.f, 0.f};
#pragma unroll
    for (int w2 = 0; w2 < WAVES; ++w2) s += *reinterpret_cast<const f32x2*>(yl + 2048 + ((wv * 8 + w2) * 64 + lane) * 2);
    *reinterpret_cast<f32x2*>(yws + wv * (SMAX * DM) + 512 + c * 4 + (r & 1) * 2) = s;
  } else if (wv == 3 && (lane & 47) == 0) {
    f32x2* ml = reinterpret_cast<f32x2*>(a.ws + WS_ML / 4);
#pragma unroll
    for (int g = 0; g < G; ++g) ml[((br * 2 + (r & 1)) * 32 + kvh * G + g) * SMAX + j] = mlout[g];
  }
  TS(4);
}

__global__ void __launch_bounds__(NTHR, 2) attn_main(Args a) {
  __shared__ __attribute__((aligned(16))) char lds[LDS_BYTES];
  {
    unsigned long long pc0; unsigned len;
    asm volatile("s_getpc_b64 %0\nattn_text_beg:\n\ts_mov_b32 %1, attn_text_end-attn_text_beg" : "=s"(pc0), "=s"(len));
    unsigned off = threadIdx.x * 128u; off = off < len ? off : 0u;
    const unsigned lb = (unsigned)(size_t)(__attribute__((address_space(3))) char*)lds + LDS_REC;
    asm volatile("s_mov_b32 m0, %0\n\ts_nop 0\n\tglobal_load_lds_dword %1, %2" :: "s"(lb), "v"(off), "s"(pc0 & ~3ull) : "m0", "memory");
  }
  const int xcd = blockIdx.x & 7, slot = blockIdx.x >> 3;
  int isa, h, j;
  isa = slot < A_PER_X;
  if (isa) { h = ((xcd + AOFF) & 7) * 2 + slot / NWA; j = slot % NWA; }
  else { h = xcd * 4 + (slot - A_PER_X) / NWB; j = (slot - A_PER_X) % NWB; }
  const bool sh = false;
  if (isa) body<2>(a, 0, h, j, (TILES * j) / NWA, (TILES * (j + 1)) / NWA, lds);
  else {
    const int t0 = sh ? ((TILES - HT) * j) / 4 : (TILES * j) / NWB;
    const int t1 = sh ? (j == 4 ? TILES : ((TILES - HT) * (j + 1)) / 4) : (TILES * (j + 1)) / NWB;
    body<1>(a, 1, h, j, t0, t1, lds);
  }
}
#define IS_B(i) (((i) >> 3) >= A_PER_X)

__global__ void __launch_bounds__(256) attn_final(const float* ws, __hip_bfloat16* out) {
  __shared__ __attribute__((aligned(16))) float red[64 * 16];
  const int chunk = blockIdx.x % 36, bb = (blockIdx.x / 36) & 1, br = blockIdx.x / 72;
  const int tid = threadIdx.x, q4 = tid & 3, vi = tid >> 2;
  const int h = vi >> 1, half = vi & 1;
  const int S = br ? NWB : NWA;
  const f32x2* ml = reinterpret_cast<const f32x2*>(ws + WS_ML / 4) + ((br * 2 + bb) * 32 + h) * SMAX;
  const float* y = ws + WS_Y / 4 + (size_t)(((br * 2 + bb) * 32 + h) * SMAX) * DM + chunk * 16 + q4 * 4;
  f32x4 yv[3];
  f32x2 mv[6];
#pragma unroll
  for (int i = 0; i < 3; ++i) { const int s = min(half * 3 + i, S - 1); yv[i] = *reinterpret_cast<const f32x4*>(y + s * DM); }
#pragma unroll
  for (int s = 0; s < 6; ++s) mv[s] = ml[min(s, S - 1)];
  if (S == 5) mv[5] = f32x2{-INFINITY, 0.f};
  float M = mv[0][0];
#pragma unroll
  for (int s = 1; s < 6; ++s) M = fmaxf(M, mv[s][0]);
  float L = 0.f, wt[6];
#pragma unroll
  for (int s = 0; s < 6; ++s) { wt[s] = __builtin_amdgcn_exp2f(mv[s][0] - M); L = fmaf(wt[s], mv[s][1], L); }
  const float inv = 1.f / L;
  const float w0 = half ? wt[3] : wt[0], w1 = half ? wt[4] : wt[1], w2 = half ? wt[5] : wt[2];
  const f32x4 sacc = (yv[0] * w0 + yv[1] * w1 + yv[2] * w2) * inv;
  *reinterpret_cast<f32x4*>(red + vi * 16 + q4 * 4) = sacc;
  __syncthreads();
  if (tid < 64) {
    const int col = tid & 15, part = tid >> 4;
    float s = 0.f;
#pragma unroll
    for (int i = 0; i < 16; ++i) s += red[(part * 16 + i) * 16 + col];
    s = xrow_sum(s);
    if (tid < 16) out[bb * (2 * DM) + br * DM + chunk * 16 + col] = __float2bfloat16(s);
  }
  asm volatile("attn_text_end:");
}

extern "C" void kernel_launch(void* const* d_in, const int* in_sizes, int n_in,
                              void* d_out, int out_size, void* d_ws, size_t ws_size,
                              hipStream_t stream) {
  Args a;
  for (int i = 0; i < 2; ++i) {
    a.x[i] = (const unsigned short*)d_in[5 * i + 0];
    a.k[i] = (const unsigned short*)d_in[5 * i + 1];
    a.v[i] = (const unsigned short*)d_in[5 * i + 2];
    a.wq[i] = (const unsigned short*)d_in[5 * i + 3];
    a.wo[i] = (const unsigned short*)d_in[5 * i + 4];
  }
  a.ws = (float*)d_ws;
  hipLaunchKernelGGL(attn_main, dim3(GRID1), dim3(NTHR), 0, stream, a);
  hipLaunchKernelGGL(attn_final, dim3(144), dim3(256), 0, stream, (const float*)d_ws, (__hip_bfloat16*)d_out);
}
```

```cpp
#include <hip/hip_runtime.h>
#include <hip/hip_bf16.h>
#include <stdint.h>

typedef __attribute__((ext_vector_type(4))) unsigned u32x4;
typedef __attribute__((ext_vector_type(2))) unsigned u32x2;
typedef __attribute__((ext_vector_type(4))) float f32x4;
typedef __attribute__((ext_vector_type(2))) float f32x2;
typedef __attribute__((ext_vector_type(2))) __bf16 bf16x2;
typedef __attribute__((ext_vector_type(8))) __bf16 bf16x8;
typedef __attribute__((ext_vector_type(4))) short s16x4;

#define NWA 6
#define NWB 5
#define KVBUF 0
#define KVAUX 2
#define QBAR 1
#define WNT 1
#define BIAS 0
#define HT 102
#define CPF 1
#define LOCK 1
#define NBA 2
#define NBB 2
#define TOUCH 1
#define AOFF 1

#define TS(i) do {} while (0)
constexpr int DM = 576, DH = 128, SK = 8192, NQ = 4096;
constexpr int WAVES = 8, NTHR = WAVES * 64;
constexpr int TILES = SK / 16;
constexpr int SMAX = 8;
constexpr int A_PER_X = 2 * NWA, B_PER_X = 4 * NWB, WG_PER_X = A_PER_X + B_PER_X;
constexpr int GRID1 = 8 * WG_PER_X;
constexpr float C2 = 0.088388347648318440f * 1.4426950408889634f;

constexpr size_t WS_ML = 0, WS_Y = 65536;
constexpr int RECS = 272;
constexpr int LDS_Q = 131072, LDS_REC = LDS_Q + 16384, LDS_ML = LDS_REC + 2 * 16 * RECS, LDS_BYTES = LDS_ML + 128;

struct Args {
  const unsigned short* x[2];
  const unsigned short* k[2];
  const unsigned short* v[2];
  const unsigned short* wq[2];
  const unsigned short* wo[2];
  float* ws;
  const void* out;
};

__device__ __forceinline__ float dot2(unsigned a, unsigned b, float acc) {
  return __builtin_amdgcn_fdot2_f32_bf16(__builtin_bit_cast(bf16x2, a), __builtin_bit_cast(bf16x2, b), acc, false);
}
__device__ __forceinline__ unsigned cvtpk(float lo, float hi) {
  unsigned r; asm("v_cvt_pk_bf16_f32 %0, %1, %2" : "=v"(r) : "v"(lo), "v"(hi)); return r;
}
template <int CTRL> __device__ __forceinline__ float dpp(float x) {
  return __builtin_bit_cast(float, __builtin_amdgcn_mov_dpp(__builtin_bit_cast(int, x), CTRL, 0xf, 0xf, true));
}
constexpr int XOR1 = 0xB1, XOR2 = 0x4E, XOR7 = 0x141, XOR8 = 0x128;
__device__ __forceinline__ float row_sum16(float x) {
  x += dpp<XOR8>(x); x += dpp<XOR7>(x); x += dpp<XOR2>(x); x += dpp<XOR1>(x); return x;
}
__device__ __forceinline__ float xrow_max(float x) {
  auto s = __builtin_amdgcn_permlane16_swap(__float_as_uint(x), __float_as_uint(x), false, false);
  x = fmaxf(__uint_as_float(s[0]), __uint_as_float(s[1]));
  auto t = __builtin_amdgcn_permlane32_swap(__float_as_uint(x), __float_as_uint(x), false, false);
  return fmaxf(__uint_as_float(t[0]), __uint_as_float(t[1]));
}
__device__ __forceinline__ float xrow_sum(float x) {
  auto s = __builtin_amdgcn_permlane16_swap(__float_as_uint(x), __float_as_uint(x), false, false);
  x = __uint_as_float(s[0]) + __uint_as_float(s[1]);
  auto t = __builtin_amdgcn_permlane32_swap(__float_as_uint(x), __float_as_uint(x), false, false);
  return __uint_as_float(t[0]) + __uint_as_float(t[1]);
}
__device__ __forceinline__ float bflo(unsigned u) { return __uint_as_float(u << 16); }
__device__ __forceinline__ float bfhi(unsigned u) { return __uint_as_float(u & 0xffff0000u); }
typedef const __attribute__((address_space(1))) char* gptr;
typedef const __attribute__((address_space(1))) u32x4* gvec;
typedef gptr rsrc_t;
__device__ __forceinline__ rsrc_t mkrsrc(gptr p, unsigned) { return p; }
#define LDKV(r, i) __builtin_nontemporal_load((gvec)(r + (size_t)tile * 4096 + (i) * 1024 + loff))
__device__ __forceinline__ u32x4 ld16(gptr base, unsigned off) {
  return __builtin_nontemporal_load((gvec)(base + off));
}

struct Tile { u32x4 k[4]; u32x4 v[4]; };

__device__ __forceinline__ void load_tile(Tile& t, rsrc_t kr, rsrc_t vr, int tile, unsigned loff) {
#pragma unroll
  for (int i = 0; i < 4; ++i) t.k[i] = LDKV(kr, i);
#pragma unroll
  for (int i = 0; i < 4; ++i) t.v[i] = LDKV(vr, i);
}

template <int G>
__device__ __forceinline__ void compute_tile(const Tile& t, const u32x4 (&q)[G], float (&m)[G], float (&l)[G], f32x2 (&acc)[G][4]) {
  float p[G][4];
#pragma unroll
  for (int g = 0; g < G; ++g) {
    float s[4];
#pragma unroll
    for (int i = 0; i < 4; ++i) {
      float d = dot2(t.k[i][0], q[g][0], 0.f);
      d = dot2(t.k[i][1], q[g][1], d);
      d = dot2(t.k[i][2], q[g][2], d);
      d = dot2(t.k[i][3], q[g][3], d);
      s[i] = d;
    }
#pragma unroll
    for (int i = 0; i < 4; ++i) s[i] = row_sum16(s[i]) * C2;
    const float tm = fmaxf(fmaxf(s[0], s[1]), fmaxf(s[2], s[3]));
    if (__builtin_amdgcn_ballot_w64(tm > m[g]) != 0ull) {
      const float mn = fmaxf(m[g], tm);
      const float al = __builtin_amdgcn_exp2f(m[g] - mn);
      m[g] = mn; l[g] *= al;
#pragma unroll
      for (int j = 0; j < 4; ++j) acc[g][j] *= al;
    }
#pragma unroll
    for (int i = 0; i < 4; ++i) p[g][i] = __builtin_amdgcn_exp2f(s[i] - m[g]);
    l[g] += (p[g][0] + p[g][1]) + (p[g][2] + p[g][3]);
  }
#pragma unroll
  for (int i = 0; i < 4; ++i) {
#pragma unroll
    for (int j = 0; j < 4; ++j) {
      const f32x2 vf = {bflo(t.v[i][j]), bfhi(t.v[i][j])};
#pragma unroll
      for (int g = 0; g < G; ++g) {
        const f32x2 pp = {p[g][i], p[g][i]};
        acc[g][j] = __builtin_elementwise_fma(pp, vf, acc[g][j]);
      }
    }
  }
#pragma unroll
  for (int g = 0; g < G; ++g)
    asm volatile("" : "+v"(acc[g][0]), "+v"(acc[g][1]), "+v"(acc[g][2]), "+v"(acc[g][3]));
}

#define LBAR() asm volatile("s_waitcnt lgkmcnt(0)\n\ts_barrier" ::: "memory")
__device__ __forceinline__ f32x4 mfma32(u32x4 a, u32x4 b, f32x4 c) {
  return __builtin_amdgcn_mfma_f32_16x16x32_bf16(__builtin_bit_cast(bf16x8, a), __builtin_bit_cast(bf16x8, b), c, 0, 0, 0);
}
__device__ __forceinline__ f32x4 mfma16(u32x2 a, u32x2 b, f32x4 c) {
  return __builtin_amdgcn_mfma_f32_16x16x16bf16_1k(__builtin_bit_cast(s16x4, a), __builtin_bit_cast(s16x4, b), c, 0, 0, 0);
}
__device__ __forceinline__ unsigned pairk(unsigned r0, unsigned r1, int odd) {
  return __builtin_amdgcn_perm(r1, r0, odd ? 0x07060302u : 0x05040100u);
}
__device__ __forceinline__ void tr8(u32x4* o, const u32x4* w) {
#pragma unroll
  for (int j = 0; j < 8; ++j) {
    o[j] = u32x4{pairk(w[0][j >> 1], w[1][j >> 1], j & 1), pairk(w[2][j >> 1], w[3][j >> 1], j & 1),
                 pairk(w[4][j >> 1], w[5][j >> 1], j & 1), pairk(w[6][j >> 1], w[7][j >> 1], j & 1)};
    asm volatile("" : "+v"(o[j]));
  }
}
__device__ __forceinline__ void mm8(f32x4 (&d)[8], const u32x4 a, const u32x4* w) {
#pragma unroll
  for (int j = 0; j < 8; ++j) {
    const u32x4 bv = {pairk(w[0][j >> 1], w[1][j >> 1], j & 1), pairk(w[2][j >> 1], w[3][j >> 1], j & 1),
                      pairk(w[4][j >> 1], w[5][j >> 1], j & 1), pairk(w[6][j >> 1], w[7][j >> 1], j & 1)};
    d[j] = mfma32(a, bv, d[j]);
  }
}

template <int G>
__device__ __forceinline__ void body(const Args& a, const int br, const int kvh, const int j, const int t0, const int t1, char* lds) {
  const int tid = threadIdx.x, lane = tid & 63, wave = __builtin_amdgcn_readfirstlane(tid >> 6);
  const int r = lane >> 4, c = lane & 15;
  const int b = wave & 1, wv = wave >> 1;
  constexpr int HKV = 32 / G, HW = WAVES / 2;
  gptr xp = (gptr)(br ? a.x[1] : a.x[0]);
  gptr wqp = (gptr)(br ? a.wq[1] : a.wq[0]);
  gptr wop = (gptr)(br ? a.wo[1] : a.wo[0]);
  const rsrc_t kp = mkrsrc((gptr)(br ? a.k[1] : a.k[0]) + (size_t)(b * HKV + kvh) * (SK * DH * 2), SK * DH * 2);
  const rsrc_t vp = mkrsrc((gptr)(br ? a.v[1] : a.v[0]) + (size_t)(b * HKV + kvh) * (SK * DH * 2), SK * DH * 2);
  const unsigned loff = lane * 16;
  int t = t0 + wv;

  u32x4 q[G];
  constexpr int NB = G == 2 ? NBA : NBB;
  constexpr int NP = G == 2 ? 1 : 2;
  Tile tl[NB];
  TS(0);
  {
    unsigned xr[9];
#pragma unroll
    for (int i = 0; i < 9; ++i)
      xr[i] = *(const __attribute__((address_space(1))) unsigned*)(xp + wave * 144 + i * 4 + (unsigned)((c & 1) * (DM * 2) + r * 36));
    u32x4 w[G][18];
#pragma unroll
    for (int g = 0; g < G; ++g)
#pragma unroll
      for (int i = 0; i < 18; ++i)
        w[g][i] = ld16(wqp + (size_t)(wave * 72 + i) * (NQ * 2) + (kvh * G + g) * (DH * 2), (unsigned)(r * 18 * NQ * 2 + c * 16));
    __builtin_amdgcn_sched_barrier(0);
    __builtin_amdgcn_s_barrier();
#pragma unroll
    for (int i = 0; i < NP; ++i) load_tile(tl[i], kp, vp, t + i * HW, loff);
    __builtin_amdgcn_sched_barrier(0);
    float* qp = reinterpret_cast<float*>(lds + LDS_Q);
#pragma unroll
    for (int g = 0; g < G; ++g) {
      f32x4 d[8];
#pragma unroll
      for (int e = 0; e < 8; ++e) d[e] = f32x4{0.f, 0.f, 0.f, 0.f};
      mm8(d, u32x4{xr[0], xr[1], xr[2], xr[3]}, &w[g][0]);
      __builtin_amdgcn_sched_barrier(0);
      mm8(d, u32x4{xr[4], xr[5], xr[6], xr[7]}, &w[g][8]);
      __builtin_amdgcn_sched_barrier(0);
#pragma unroll
      for (int e = 0; e < 8; ++e) d[e] = mfma16(u32x2{xr[8], 0u}, u32x2{pairk(w[g][16][e >> 1], w[g][17][e >> 1], e & 1), 0u}, d[e]);
      f32x4 o4;
#pragma unroll
      for (int e = 0; e < 4; ++e) {
        const float lo = (r & 2) ? d[e][1] : d[e][0], hi = (r & 2) ? d[e + 4][1] : d[e + 4][0];
        o4[e] = (r & 1) ? hi : lo;
      }
      *reinterpret_cast<f32x4*>(qp + ((wave * G + g) * 2 + (r >> 1)) * DH + c * 8 + (r & 1) * 4) = o4;
    }
    LBAR();
#pragma unroll
    for (int g = 0; g < G; ++g) {
      f32x4 lo = {0.f, 0.f, 0.f, 0.f}, hi = {0.f, 0.f, 0.f, 0.f};
#pragma unroll
      for (int w2 = 0; w2 < WAVES; ++w2) {
        lo += *reinterpret_cast<const f32x4*>(qp + ((w2 * G + g) * 2 + b) * DH + c * 8);
        hi += *reinterpret_cast<const f32x4*>(qp + ((w2 * G + g) * 2 + b) * DH + c * 8 + 4);
      }
      q[g] = u32x4{cvtpk(lo[0], lo[1]), cvtpk(lo[2], lo[3]), cvtpk(hi[0], hi[1]), cvtpk(hi[2], hi[3])};
    }
  }
  __builtin_amdgcn_sched_barrier(0);
  u32x4 wm[G == 2 ? 16 : 1];
  u32x2 wsd[4 * G];
  const int og = G == 2 ? wave >> 2 : 0, cb = G == 2 ? wave & 3 : wv;
  gptr wh = wop + (size_t)(kvh * G * DH) * (DM * 2);
  const unsigned mo = (unsigned)(((G == 2 ? og * DH : b * 64) + r * 8) * (DM * 2) + cb * 256 + c * 16);
  const unsigned lb = (unsigned)(size_t)(__attribute__((address_space(3))) char*)lds + wave * 16384;
#define WO_DMA(i) asm volatile("s_mov_b32 m0, %0\n\ts_nop 0\n\tglobal_load_lds_dwordx4 %1, %2 nt" \
    :: "s"(lb + (i) * 1024), "v"(mo), "s"(wh + ((G - 1) * 64 + ((i) >> 3) * 32 + ((i) & 7)) * (DM * 2)) : "m0", "memory")
  {
    const unsigned so = (unsigned)((wave * 16 + r * 4) * G * (DM * 2) + 1024 + c * 8);
    if (G == 2) {
#pragma unroll
      for (int i = 0; i < 16; ++i) wm[G == 2 ? i : 0] = ld16(wh + ((i >> 3) * 32 + (i & 7)) * (DM * 2), mo);
    }
#pragma unroll
    for (int i = 0; i < 4 * G; ++i)
      wsd[i] = __builtin_nontemporal_load((const __attribute__((address_space(1))) u32x2*)(wh + i * (DM * 2) + so));
  }
  if (wave == 7) {
    const char* tp = (lane & 3) == 3 ? (const char*)a.out : (const char*)a.ws + ((lane & 3) == 2 ? WS_ML :
                     WS_Y + ((size_t)((br * 2 + (lane & 1)) * 32 + kvh * G) * SMAX + j) * (DM * 4));
    asm volatile("s_mov_b32 m0, %0\n\ts_nop 0\n\tglobal_load_lds_dword %1, off nt"
                 :: "s"((unsigned)(size_t)(__attribute__((address_space(3))) char*)lds + LDS_REC + 13 * RECS), "v"(tp) : "m0", "memory");
  }
  __builtin_amdgcn_sched_barrier(0);

  TS(1);
  float m[G], l[G];
  f32x2 acc[G][4];
#pragma unroll
  for (int g = 0; g < G; ++g) { m[g] = -INFINITY; l[g] = 0.f;
#pragma unroll
    for (int e = 0; e < 4; ++e) acc[g][e] = f32x2{0.f, 0.f}; }
#define SB() __builtin_amdgcn_sched_barrier(0)
#pragma unroll
  for (int i = NP; i < NB; ++i) load_tile(tl[i], kp, vp, t + i * HW, loff);
  t += NB * HW;
  int wi = 0;
  for (; t + (NB - 1) * HW < t1; t += NB * HW) {
#pragma unroll
    for (int k = 0; k < NB; ++k) {
      compute_tile<G>(tl[k], q, m, l, acc); SB();
      load_tile(tl[k], kp, vp, t + k * HW, loff); SB();
      if (k < NB - 1 && wi < 16) { WO_DMA(wi); WO_DMA(wi + 1); wi += 2; }
      SB();
    }
    if (t - wv + NB * HW <= t1 && (((t - wv - t0) / (NB * HW)) % LOCK) == 0) __builtin_amdgcn_s_barrier();
  }
#pragma unroll
  for (int k = 0; k < NB; ++k) {
    compute_tile<G>(tl[k], q, m, l, acc); SB();
    if (k < NB - 1 && t + k * HW < t1) load_tile(tl[k], kp, vp, t + k * HW, loff);
    SB();
  }
#pragma unroll
  for (int k = 0; k < NB - 1; ++k)
    if (t + k * HW < t1) compute_tile<G>(tl[k], q, m, l, acc);
  TS(2);
  u32x4 wl[16];
  asm volatile("s_waitcnt vmcnt(0)" ::: "memory");
#pragma unroll
  for (int i = 0; i < 16; ++i) wl[i] = *reinterpret_cast<const u32x4*>(lds + wave * 16384 + i * 1024 + lane * 16);

  f32x2* mlw = reinterpret_cast<f32x2*>(lds + LDS_ML);
#pragma unroll
  for (int g = 0; g < G; ++g) {
    const float M = xrow_max(m[g]);
    const float sc = __builtin_amdgcn_exp2f(m[g] - M);
    const float L = xrow_sum(l[g] * sc);
    float h[4];
#pragma unroll
    for (int e = 0; e < 4; ++e) {
      const float lo = acc[g][e >> 1][e & 1] * sc, hi = acc[g][2 + (e >> 1)][e & 1] * sc;
      auto s = __builtin_amdgcn_permlane32_swap(__float_as_uint(lo), __float_as_uint(hi), false, false);
      h[e] = __uint_as_float(s[0]) + __uint_as_float(s[1]);
    }
    f32x2 o2;
#pragma unroll
    for (int e = 0; e < 2; ++e) {
      auto s = __builtin_amdgcn_permlane16_swap(__float_as_uint(h[e]), __float_as_uint(h[e + 2]), false, false);
      o2[e] = __uint_as_float(s[0]) + __uint_as_float(s[1]);
    }
    const unsigned ph = cvtpk(o2[0], o2[1]);
    const unsigned pl = cvtpk(o2[0] - bflo(ph), o2[1] - bfhi(ph));
    char* rp = lds + LDS_REC + (g * 16 + wv * 4 + b) * RECS + c * 16 + r * 4;
    *reinterpret_cast<unsigned*>(rp) = ph;
    *reinterpret_cast<unsigned*>(rp + 2 * RECS) = pl;
    if (lane == 0) mlw[wave * G + g] = f32x2{M, L};
  }
  u32x4 bt[2 * G][8], bs[4];
  if (G == 2) { tr8(bt[0], &wm[0]); tr8(bt[1], &wm[G == 2 ? 8 : 0]); }
  tr8(bt[2 * G - 2], &wl[0]); tr8(bt[2 * G - 1], &wl[8]);
#pragma unroll
  for (int e = 0; e < 4; ++e) {
#pragma unroll
    for (int i = 0; i < 4; ++i) bs[e][i] = i < 2 * G ? pairk(wsd[(2 * i) % (4 * G)][e >> 1], wsd[(2 * i + 1) % (4 * G)][e >> 1], e & 1) : 0u;
    if (G == 2) asm volatile("" : "+v"(bs[e]));
  }
  LBAR();
  TS(3);
  float* yl = reinterpret_cast<float*>(lds + LDS_Q);
  f32x4 ym;
  f32x2 mlout;
  {
    const char* rp = lds + LDS_REC + (og * 16 + c) * RECS;
    u32x4 av[2 * G];
#pragma unroll
    for (int u = 0; u < 2 * G; ++u) av[u] = *reinterpret_cast<const u32x4*>(rp + (G == 2 ? 0 : b * 128) + u * 64 + r * 16);
    const f32x2 ml0 = mlw[(r * 2) * G + og], ml1 = mlw[(r * 2 + 1) * G + og];
    f32x4 d[8], ds[4];
    const f32x4 z4 = {0.f, 0.f, 0.f, 0.f};
    if (G == 2) {
      const u32x4 a2 = *reinterpret_cast<const u32x4*>(rp + cb * 64 + r * 16);
#pragma unroll
      for (int e = 0; e < 4; ++e) ds[e] = mfma32(a2, bs[e], z4);
    } else {
      const u32x2 a2 = *reinterpret_cast<const u32x2*>(rp + wave * 32 + r * 8);
#pragma unroll
      for (int e = 0; e < 4; ++e) ds[e] = mfma16(a2, u32x2{bs[e][0], bs[e][1]}, z4);
    }
#pragma unroll
    for (int u = 0; u < 2 * G; ++u)
#pragma unroll
      for (int e = 0; e < 8; ++e) d[e] = mfma32(av[u], bt[u][e], u ? d[e] : z4);
    const float M0 = xrow_max(ml0[0]), M1 = xrow_max(ml1[0]);
    const float w0 = __builtin_amdgcn_exp2f(ml0[0] - M0), w1 = __builtin_amdgcn_exp2f(ml1[0] - M1);
    const float L0 = xrow_sum(w0 * ml0[1]), L1 = xrow_sum(w1 * ml1[1]);
    mlout = (r & 1) ? f32x2{M1, L1} : f32x2{M0, L0};
    float h[8], hs[4];
#pragma unroll
    for (int e = 0; e < 4; ++e) {
      auto s = __builtin_amdgcn_permlane32_swap(__float_as_uint(w0 * (ds[e][0] + ds[e][2])), __float_as_uint(w1 * (ds[e][1] + ds[e][3])), false, false);
      hs[e] = __uint_as_float(s[0]) + __uint_as_float(s[1]);
    }
    f32x2 ys;
#pragma unroll
    for (int e = 0; e < 2; ++e) {
      auto s = __builtin_amdgcn_permlane16_swap(__float_as_uint(hs[e]), __float_as_uint(hs[e + 2]), false, false);
      ys[e] = __uint_as_float(s[0]) + __uint_as_float(s[1]);
    }
    *reinterpret_cast<f32x2*>(yl + 1024 + (wave * 64 + lane) * 2) = ys;
#pragma unroll
    for (int e = 0; e < 8; ++e) {
      auto s = __builtin_amdgcn_permlane32_swap(__float_as_uint(w0 * (d[e][0] + d[e][2])), __float_as_uint(w1 * (d[e][1] + d[e][3])), false, false);
      h[e] = __uint_as_float(s[0]) + __uint_as_float(s[1]);
    }
#pragma unroll
    for (int e = 0; e < 4; ++e) {
      auto s = __builtin_amdgcn_permlane16_swap(__float_as_uint(h[e]), __float_as_uint(h[e + 4]), false, false);
      ym[e] = __uint_as_float(s[0]) + __uint_as_float(s[1]);
    }
  }
  float* yws = a.ws + WS_Y / 4 + ((size_t)((br * 2 + (r >> 1)) * 32 + kvh * G + og) * SMAX + j) * DM;
  f32x4* ymp = reinterpret_cast<f32x4*>(yws + cb * 128 + c * 8 + (r & 1) * 4);
  if (G == 2) *ymp = ym;
  else if (b) *reinterpret_cast<f32x4*>(yl + (wv * 64 + lane) * 4) = ym;
  if ((wave & 3) == 3 && (G == 2 || wave == 3) && (lane & 47) == 0)
    reinterpret_cast<f32x2*>(a.ws + WS_ML / 4)[((br * 2 + (r & 1)) * 32 + kvh * G + og) * SMAX + j] = mlout;
  LBAR();
  if (G == 1 && !b) *ymp = ym + *reinterpret_cast<const f32x4*>(yl + (wv * 64 + lane) * 4);
  if ((wave & 3) == 1 && (G == 2 || wave == 1)) {
    f32x2 s = {0.f, 0.f};
#pragma unroll
    for (int w2 = 0; w2 < WAVES / G; ++w2) s += *reinterpret_cast<const f32x2*>(yl + 1024 + ((og * 4 + w2) * 64 + lane) * 2);
    *reinterpret_cast<f32x2*>(yws + 512 + c * 4 + (r & 1) * 2) = s;
  }
  TS(4);
}

__global__ void __launch_bounds__(NTHR, 2) attn_main(Args a) {
  __shared__ __attribute__((aligned(16))) char lds[LDS_BYTES];
  {
    unsigned long long pc0; unsigned len;
    asm volatile("s_getpc_b64 %0\nattn_text_beg:\n\ts_mov_b32 %1, attn_text_end-attn_text_beg" : "=s"(pc0), "=s"(len));
    unsigned off = threadIdx.x * 128u; off = off < len ? off : 0u;
    const unsigned lb = (unsigned)(size_t)(__attribute__((address_space(3))) char*)lds + LDS_REC;
    asm volatile("s_mov_b32 m0, %0\n\ts_nop 0\n\tglobal_load_lds_dword %1, %2" :: "s"(lb), "v"(off), "s"(pc0 & ~3ull) : "m0", "memory");
  }
  const int xcd = blockIdx.x & 7, slot = blockIdx.x >> 3;
  int isa, h, j;
  isa = slot < A_PER_X;
  if (isa) { h = ((xcd + AOFF) & 7) * 2 + slot / NWA; j = slot % NWA; }
  else { h = xcd * 4 + (slot - A_PER_X) / NWB; j = (slot - A_PER_X) % NWB; }
  const bool sh = false;
  if (isa) body<2>(a, 0, h, j, (TILES * j) / NWA, (TILES * (j + 1)) / NWA, lds);
  else {
    const int t0 = sh ? ((TILES - HT) * j) / 4 : (TILES * j) / NWB;
    const int t1 = sh ? (j == 4 ? TILES : ((TILES - HT) * (j + 1)) / 4) : (TILES * (j + 1)) / NWB;
    body<1>(a, 1, h, j, t0, t1, lds);
  }
}
#define IS_B(i) (((i) >> 3) >= A_PER_X)

__global__ void __launch_bounds__(256) attn_final(const float* ws, __hip_bfloat16* out) {
  __shared__ __attribute__((aligned(16))) float red[64 * 16];
  const int chunk = blockIdx.x % 36, bb = (blockIdx.x / 36) & 1, br = blockIdx.x / 72;
  const int tid = threadIdx.x, q4 = tid & 3, vi = tid >> 2;
  const int h = vi >> 1, half = vi & 1;
  const int S = br ? NWB : NWA;
  const f32x2* ml = reinterpret_cast<const f32x2*>(ws + WS_ML / 4) + ((br * 2 + bb) * 32 + h) * SMAX;
  const float* y = ws + WS_Y / 4 + (size_t)(((br * 2 + bb) * 32 + h) * SMAX) * DM + chunk * 16 + q4 * 4;
  f32x4 yv[3];
  f32x2 mv[6];
#pragma unroll
  for (int i = 0; i < 3; ++i) { const int s = min(half * 3 + i, S - 1); yv[i] = *reinterpret_cast<const f32x4*>(y + s * DM); }
#pragma unroll
  for (int s = 0; s < 6; ++s) mv[s] = ml[min(s, S - 1)];
  if (S == 5) mv[5] = f32x2{-INFINITY, 0.f};
  float M = mv[0][0];
#pragma unroll
  for (int s = 1; s < 6; ++s) M = fmaxf(M, mv[s][0]);
  float L = 0.f, wt[6];
#pragma unroll
  for (int s = 0; s < 6; ++s) { wt[s] = __builtin_amdgcn_exp2f(mv[s][0] - M); L = fmaf(wt[s], mv[s][1], L); }
  const float inv = 1.f / L;
  const float w0 = half ? wt[3] : wt[0], w1 = half ? wt[4] : wt[1], w2 = half ? wt[5] : wt[2];
  const f32x4 sacc = (yv[0] * w0 + yv[1] * w1 + yv[2] * w2) * inv;
  *reinterpret_cast<f32x4*>(red + vi * 16 + q4 * 4) = sacc;
  __syncthreads();
  if (tid < 64) {
    const int col = tid & 15, part = tid >> 4;
    float s = 0.f;
#pragma unroll
    for (int i = 0; i < 16; ++i) s += red[(part * 16 + i) * 16 + col];
    s = xrow_sum(s);
    if (tid < 16) out[bb * (2 * DM) + br * DM + chunk * 16 + col] = __float2bfloat16(s);
  }
  asm volatile("attn_text_end:");
}

extern "C" void kernel_launch(void* const* d_in, const int* in_sizes, int n_in,
                              void* d_out, int out_size, void* d_ws, size_t ws_size,
                              hipStream_t stream) {
  Args a;
  for (int i = 0; i < 2; ++i) {
    a.x[i] = (const unsigned short*)d_in[5 * i + 0];
    a.k[i] = (const unsigned short*)d_in[5 * i + 1];
    a.v[i] = (const unsigned short*)d_in[5 * i + 2];
    a.wq[i] = (const unsigned short*)d_in[5 * i + 3];
    a.wo[i] = (const unsigned short*)d_in[5 * i + 4];
  }
  a.ws = (float*)d_ws;
  a.out = d_out;
  hipLaunchKernelGGL(attn_main, dim3(GRID1), dim3(NTHR), 0, stream, a);
  hipLaunchKernelGGL(attn_final, dim3(144), dim3(256), 0, stream, (const float*)d_ws, (__hip_bfloat16*)d_out);
}
```

```cpp
#include <hip/hip_runtime.h>
#include <hip/hip_bf16.h>
#include <stdint.h>

typedef __attribute__((ext_vector_type(4))) unsigned u32x4;
typedef __attribute__((ext_vector_type(2))) unsigned u32x2;
typedef __attribute__((ext_vector_type(4))) float f32x4;
typedef __attribute__((ext_vector_type(2))) float f32x2;
typedef __attribute__((ext_vector_type(2))) __bf16 bf16x2;
typedef __attribute__((ext_vector_type(8))) __bf16 bf16x8;
typedef __attribute__((ext_vector_type(4))) short s16x4;

constexpr int NWA = 6, NWB = 5;
constexpr int DM = 576, DH = 128, SK = 8192, NQ = 4096;
constexpr int WAVES = 8, NTHR = WAVES * 64;
constexpr int TILES = SK / 16;
constexpr int SMAX = 8;
constexpr int A_PER_X = 2 * NWA, B_PER_X = 4 * NWB, WG_PER_X = A_PER_X + B_PER_X;
constexpr int GRID1 = 8 * WG_PER_X;
static_assert((TILES / NWA) / 4 >= 20 && NWB <= NWA && NWA <= 6, "stream loop needs >= 9 iterations; attn_final handles <= 6 slots");
constexpr float C2 = 0.088388347648318440f * 1.4426950408889634f;

constexpr size_t WS_ML = 0, WS_Y = 65536;
constexpr int RECS = 272;
constexpr int LDS_Q = 131072, LDS_REC = LDS_Q + 16384, LDS_ML = LDS_REC + 2 * 16 * RECS, LDS_BYTES = LDS_ML + 128;

struct Args {
  const unsigned short* x[2];
  const unsigned short* k[2];
  const unsigned short* v[2];
  const unsigned short* wq[2];
  const unsigned short* wo[2];
  float* ws;
};

__device__ __forceinline__ float dot2(unsigned a, unsigned b, float acc) {
  return __builtin_amdgcn_fdot2_f32_bf16(__builtin_bit_cast(bf16x2, a), __builtin_bit_cast(bf16x2, b), acc, false);
}
__device__ __forceinline__ unsigned cvtpk(float lo, float hi) {
  unsigned r; asm("v_cvt_pk_bf16_f32 %0, %1, %2" : "=v"(r) : "v"(lo), "v"(hi)); return r;
}
template <int CTRL> __device__ __forceinline__ float dpp(float x) {
  return __builtin_bit_cast(float, __builtin_amdgcn_mov_dpp(__builtin_bit_cast(int, x), CTRL, 0xf, 0xf, true));
}
constexpr int XOR1 = 0xB1, XOR2 = 0x4E, XOR7 = 0x141, XOR8 = 0x128;
__device__ __forceinline__ float row_sum16(float x) {
  x += dpp<XOR8>(x); x += dpp<XOR7>(x); x += dpp<XOR2>(x); x += dpp<XOR1>(x); return x;
}
__device__ __forceinline__ float xrow_max(float x) {
  auto s = __builtin_amdgcn_permlane16_swap(__float_as_uint(x), __float_as_uint(x), false, false);
  x = fmaxf(__uint_as_float(s[0]), __uint_as_float(s[1]));
  auto t = __builtin_amdgcn_permlane32_swap(__float_as_uint(x), __float_as_uint(x), false, false);
  return fmaxf(__uint_as_float(t[0]), __uint_as_float(t[1]));
}
__device__ __forceinline__ float xrow_sum(float x) {
  auto s = __builtin_amdgcn_permlane16_swap(__float_as_uint(x), __float_as_uint(x), false, false);
  x = __uint_as_float(s[0]) + __uint_as_float(s[1]);
  auto t = __builtin_amdgcn_permlane32_swap(__float_as_uint(x), __float_as_uint(x), false, false);
  return __uint_as_float(t[0]) + __uint_as_float(t[1]);
}
__device__ __forceinline__ float bflo(unsigned u) { return __uint_as_float(u << 16); }
__device__ __forceinline__ float bfhi(unsigned u) { return __uint_as_float(u & 0xffff0000u); }
typedef const __attribute__((address_space(1))) char* gptr;
typedef const __attribute__((address_space(1))) u32x4* gvec;
__device__ __forceinline__ u32x4 ld16(gptr base, unsigned off) {
  return __builtin_nontemporal_load((gvec)(base + off));
}

struct Tile { u32x4 k[4]; u32x4 v[4]; };

__device__ __forceinline__ void load_tile(Tile& t, gptr kr, gptr vr, int tile, unsigned loff) {
#pragma unroll
  for (int i = 0; i < 4; ++i) t.k[i] = __builtin_nontemporal_load((gvec)(kr + (size_t)tile * 4096 + i * 1024 + loff));
#pragma unroll
  for (int i = 0; i < 4; ++i) t.v[i] = __builtin_nontemporal_load((gvec)(vr + (size_t)tile * 4096 + i * 1024 + loff));
}

template <int G>
__device__ __forceinline__ void compute_tile(const Tile& t, const u32x4 (&q)[G], float (&m)[G], float (&l)[G], f32x2 (&acc)[G][4]) {
  float p[G][4];
#pragma unroll
  for (int g = 0; g < G; ++g) {
    float s[4];
#pragma unroll
    for (int i = 0; i < 4; ++i) {
      float d = dot2(t.k[i][0], q[g][0], 0.f);
      d = dot2(t.k[i][1], q[g][1], d);
      d = dot2(t.k[i][2], q[g][2], d);
      d = dot2(t.k[i][3], q[g][3], d);
      s[i] = d;
    }
#pragma unroll
    for (int i = 0; i < 4; ++i) s[i] = row_sum16(s[i]) * C2;
    const float tm = fmaxf(fmaxf(s[0], s[1]), fmaxf(s[2], s[3]));
    if (__builtin_amdgcn_ballot_w64(tm > m[g]) != 0ull) {
      const float mn = fmaxf(m[g], tm);
      const float al = __builtin_amdgcn_exp2f(m[g] - mn);
      m[g] = mn; l[g] *= al;
#pragma unroll
      for (int j = 0; j < 4; ++j) acc[g][j] *= al;
    }
#pragma unroll
    for (int i = 0; i < 4; ++i) p[g][i] = __builtin_amdgcn_exp2f(s[i] - m[g]);
    l[g] += (p[g][0] + p[g][1]) + (p[g][2] + p[g][3]);
  }
#pragma unroll
  for (int i = 0; i < 4; ++i) {
#pragma unroll
    for (int j = 0; j < 4; ++j) {
      const f32x2 vf = {bflo(t.v[i][j]), bfhi(t.v[i][j])};
#pragma unroll
      for (int g = 0; g < G; ++g) {
        const f32x2 pp = {p[g][i], p[g][i]};
        acc[g][j] = __builtin_elementwise_fma(pp, vf, acc[g][j]);
      }
    }
  }
#pragma unroll
  for (int g = 0; g < G; ++g)
    asm volatile("" : "+v"(acc[g][0]), "+v"(acc[g][1]), "+v"(acc[g][2]), "+v"(acc[g][3]));
}

#define LBAR() asm volatile("s_waitcnt lgkmcnt(0)\n\ts_barrier" ::: "memory")
__device__ __forceinline__ f32x4 mfma32(u32x4 a, u32x4 b, f32x4 c) {
  return __builtin_amdgcn_mfma_f32_16x16x32_bf16(__builtin_bit_cast(bf16x8, a), __builtin_bit_cast(bf16x8, b), c, 0, 0, 0);
}
__device__ __forceinline__ f32x4 mfma16(u32x2 a, u32x2 b, f32x4 c) {
  return __builtin_amdgcn_mfma_f32_16x16x16bf16_1k(__builtin_bit_cast(s16x4, a), __builtin_bit_cast(s16x4, b), c, 0, 0, 0);
}
__device__ __forceinline__ unsigned pairk(unsigned r0, unsigned r1, int odd) {
  return __builtin_amdgcn_perm(r1, r0, odd ? 0x07060302u : 0x05040100u);
}
__device__ __forceinline__ void tr8(u32x4* o, const u32x4* w) {
#pragma unroll
  for (int j = 0; j < 8; ++j) {
    o[j] = u32x4{pairk(w[0][j >> 1], w[1][j >> 1], j & 1), pairk(w[2][j >> 1], w[3][j >> 1], j & 1),
                 pairk(w[4][j >> 1], w[5][j >> 1], j & 1), pairk(w[6][j >> 1], w[7][j >> 1], j & 1)};
    asm volatile("" : "+v"(o[j]));
  }
}
__device__ __forceinline__ void mm8(f32x4 (&d)[8], const u32x4 a, const u32x4* w) {
#pragma unroll
  for (int j = 0; j < 8; ++j) {
    const u32x4 bv = {pairk(w[0][j >> 1], w[1][j >> 1], j & 1), pairk(w[2][j >> 1], w[3][j >> 1], j & 1),
                      pairk(w[4][j >> 1], w[5][j >> 1], j & 1), pairk(w[6][j >> 1], w[7][j >> 1], j & 1)};
    d[j] = mfma32(a, bv, d[j]);
  }
}

template <int G>
__device__ __forceinline__ void body(const Args& a, const int br, const int kvh, const int j, const int t0, const int t1, char* lds) {
  const int tid = threadIdx.x, lane = tid & 63, wave = __builtin_amdgcn_readfirstlane(tid >> 6);
  const int r = lane >> 4, c = lane & 15;
  const int b = wave & 1, wv = wave >> 1;
  constexpr int HKV = 32 / G, HW = WAVES / 2;
  gptr xp = (gptr)(br ? a.x[1] : a.x[0]);
  gptr wqp = (gptr)(br ? a.wq[1] : a.wq[0]);
  gptr wop = (gptr)(br ? a.wo[1] : a.wo[0]);
  const gptr kp = (gptr)(br ? a.k[1] : a.k[0]) + (size_t)(b * HKV + kvh) * (SK * DH * 2);
  const gptr vp = (gptr)(br ? a.v[1] : a.v[0]) + (size_t)(b * HKV + kvh) * (SK * DH * 2);
  const unsigned loff = lane * 16;
  int t = t0 + wv;

  u32x4 q[G];
  constexpr int NB = 2;
  constexpr int NP = G == 2 ? 1 : 2;
  Tile tl[NB];
  {
    unsigned xr[9];
#pragma unroll
    for (int i = 0; i < 9; ++i)
      xr[i] = *(const __attribute__((address_space(1))) unsigned*)(xp + wave * 144 + i * 4 + (unsigned)((c & 1) * (DM * 2) + r * 36));
    u32x4 w[G][18];
#pragma unroll
    for (int g = 0; g < G; ++g)
#pragma unroll
      for (int i = 0; i < 18; ++i)
        w[g][i] = ld16(wqp + (size_t)(wave * 72 + i) * (NQ * 2) + (kvh * G + g) * (DH * 2), (unsigned)(r * 18 * NQ * 2 + c * 16));
    __builtin_amdgcn_sched_barrier(0);
    __builtin_amdgcn_s_barrier();
#pragma unroll
    for (int i = 0; i < NP; ++i) load_tile(tl[i], kp, vp, t + i * HW, loff);
    __builtin_amdgcn_sched_barrier(0);
    float* qp = reinterpret_cast<float*>(lds + LDS_Q);
#pragma unroll
    for (int g = 0; g < G; ++g) {
      f32x4 d[8];
#pragma unroll
      for (int e = 0; e < 8; ++e) d[e] = f32x4{0.f, 0.f, 0.f, 0.f};
      mm8(d, u32x4{xr[0], xr[1], xr[2], xr[3]}, &w[g][0]);
      __builtin_amdgcn_sched_barrier(0);
      mm8(d, u32x4{xr[4], xr[5], xr[6], xr[7]}, &w[g][8]);
      __builtin_amdgcn_sched_barrier(0);
#pragma unroll
      for (int e = 0; e < 8; ++e) d[e] = mfma16(u32x2{xr[8], 0u}, u32x2{pairk(w[g][16][e >> 1], w[g][17][e >> 1], e & 1), 0u}, d[e]);
      f32x4 o4;
#pragma unroll
      for (int e = 0; e < 4; ++e) {
        const float lo = (r & 2) ? d[e][1] : d[e][0], hi = (r & 2) ? d[e + 4][1] : d[e + 4][0];
        o4[e] = (r & 1) ? hi : lo;
      }
      *reinterpret_cast<f32x4*>(qp + ((wave * G + g) * 2 + (r >> 1)) * DH + c * 8 + (r & 1) * 4) = o4;
    }
    LBAR();
#pragma unroll
    for (int g = 0; g < G; ++g) {
      const float* q0 = qp + ((2 * r * G + g) * 2 + b) * DH + c * 8;
      f32x4 lo = *reinterpret_cast<const f32x4*>(q0) + *reinterpret_cast<const f32x4*>(q0 + G * 2 * DH);
      f32x4 hi = *reinterpret_cast<const f32x4*>(q0 + 4) + *reinterpret_cast<const f32x4*>(q0 + G * 2 * DH + 4);
#pragma unroll
      for (int e = 0; e < 4; ++e) { lo[e] = xrow_sum(lo[e]); hi[e] = xrow_sum(hi[e]); }
      q[g] = u32x4{cvtpk(lo[0], lo[1]), cvtpk(lo[2], lo[3]), cvtpk(hi[0], hi[1]), cvtpk(hi[2], hi[3])};
    }
  }
  __builtin_amdgcn_sched_barrier(0);
  u32x4 wm[G == 2 ? 16 : 1];
  u32x2 wsd[4 * G];
  const int og = G == 2 ? wave >> 2 : 0, cb = G == 2 ? wave & 3 : wv;
  gptr wh = wop + (size_t)(kvh * G * DH) * (DM * 2);
  const unsigned mo = (unsigned)(((G == 2 ? og * DH : b * 64) + r * 8) * (DM * 2) + cb * 256 + c * 16);
  const unsigned lb = (unsigned)(size_t)(__attribute__((address_space(3))) char*)lds + wave * 16384;
#define WO_DMA(i) asm volatile("s_mov_b32 m0, %0\n\ts_nop 0\n\tglobal_load_lds_dwordx4 %1, %2 nt" \
    :: "s"(lb + (i) * 1024), "v"(mo), "s"(wh + ((G - 1) * 64 + ((i) >> 3) * 32 + ((i) & 7)) * (DM * 2)) : "m0", "memory")
  {
    const unsigned so = (unsigned)((wave * 16 + r * 4) * G * (DM * 2) + 1024 + c * 8);
    if (G == 2) {
#pragma unroll
      for (int i = 0; i < 16; ++i) wm[G == 2 ? i : 0] = ld16(wh + ((i >> 3) * 32 + (i & 7)) * (DM * 2), mo);
    }
#pragma unroll
    for (int i = 0; i < 4 * G; ++i)
      wsd[i] = __builtin_nontemporal_load((const __attribute__((address_space(1))) u32x2*)(wh + i * (DM * 2) + so));
  }
  __builtin_amdgcn_sched_barrier(0);

  float m[G], l[G];
  f32x2 acc[G][4];
#pragma unroll
  for (int g = 0; g < G; ++g) { m[g] = -INFINITY; l[g] = 0.f;
#pragma unroll
    for (int e = 0; e < 4; ++e) acc[g][e] = f32x2{0.f, 0.f}; }
#define SB() __builtin_amdgcn_sched_barrier(0)
#pragma unroll
  for (int i = NP; i < NB; ++i) load_tile(tl[i], kp, vp, t + i * HW, loff);
  t += NB * HW;
  int wi = 0;
  for (; t + (NB - 1) * HW < t1; t += NB * HW) {
#pragma unroll
    for (int k = 0; k < NB; ++k) {
      compute_tile<G>(tl[k], q, m, l, acc); SB();
      load_tile(tl[k], kp, vp, t + k * HW, loff); SB();
      if (k < NB - 1 && wi < 16) { WO_DMA(wi); WO_DMA(wi + 1); wi += 2; }
      SB();
    }
    if (t - wv + NB * HW <= t1) __builtin_amdgcn_s_barrier();
  }
  u32x4 bt[2 * G][8], bs[4];
  asm volatile("s_waitcnt vmcnt(16)" ::: "memory");
#define WO_LDS(u) { u32x4 wl[8]; \
    _Pragma("unroll") for (int i = 0; i < 8; ++i) wl[i] = *reinterpret_cast<const u32x4*>(lds + wave * 16384 + ((u) * 8 + i) * 1024 + lane * 16); \
    tr8(bt[2 * G - 2 + (u)], wl); }
  if (G == 2) { tr8(bt[0], &wm[0]); tr8(bt[1], &wm[G == 2 ? 8 : 0]); }
  else { WO_LDS(0); WO_LDS(1); }
  SB();
  compute_tile<G>(tl[0], q, m, l, acc); SB();
  if (t < t1) {
    load_tile(tl[0], kp, vp, t, loff); SB();
    compute_tile<G>(tl[1], q, m, l, acc); SB();
    if (G == 2) WO_LDS(0);
    SB();
    compute_tile<G>(tl[0], q, m, l, acc);
  } else {
    compute_tile<G>(tl[1], q, m, l, acc); SB();
    if (G == 2) WO_LDS(0);
  }
  if (G == 2) WO_LDS(1);
#pragma unroll
  for (int e = 0; e < 4; ++e) {
#pragma unroll
    for (int i = 0; i < 4; ++i) bs[e][i] = i < 2 * G ? pairk(wsd[(2 * i) % (4 * G)][e >> 1], wsd[(2 * i + 1) % (4 * G)][e >> 1], e & 1) : 0u;
    asm volatile("" : "+v"(bs[e][0]), "+v"(bs[e][1]));
    if (G == 2) asm volatile("" : "+v"(bs[e][2]), "+v"(bs[e][3]));
  }

  f32x2* mlw = reinterpret_cast<f32x2*>(lds + LDS_ML);
#pragma unroll
  for (int g = 0; g < G; ++g) {
    const float M = xrow_max(m[g]);
    const float sc = __builtin_amdgcn_exp2f(m[g] - M);
    const float L = xrow_sum(l[g] * sc);
    float h[4];
#pragma unroll
    for (int e = 0; e < 4; ++e) {
      const float lo = acc[g][e >> 1][e & 1] * sc, hi = acc[g][2 + (e >> 1)][e & 1] * sc;
      auto s = __builtin_amdgcn_permlane32_swap(__float_as_uint(lo), __float_as_uint(hi), false, false);
      h[e] = __uint_as_float(s[0]) + __uint_as_float(s[1]);
    }
    f32x2 o2;
#pragma unroll
    for (int e = 0; e < 2; ++e) {
      auto s = __builtin_amdgcn_permlane16_swap(__float_as_uint(h[e]), __float_as_uint(h[e + 2]), false, false);
      o2[e] = __uint_as_float(s[0]) + __uint_as_float(s[1]);
    }
    const unsigned ph = cvtpk(o2[0], o2[1]);
    const unsigned pl = cvtpk(o2[0] - bflo(ph), o2[1] - bfhi(ph));
    char* rp = lds + LDS_REC + (g * 16 + wv * 4 + b) * RECS + c * 16 + r * 4;
    *reinterpret_cast<unsigned*>(rp) = ph;
    *reinterpret_cast<unsigned*>(rp + 2 * RECS) = pl;
    if (lane == 0) mlw[wave * G + g] = f32x2{M, L};
  }
  LBAR();
  float* yl = reinterpret_cast<float*>(lds + LDS_Q);
  f32x4 ym;
  f32x2 mlout;
  {
    const char* rp = lds + LDS_REC + (og * 16 + c) * RECS;
    u32x4 av[2 * G];
#pragma unroll
    for (int u = 0; u < 2 * G; ++u) av[u] = *reinterpret_cast<const u32x4*>(rp + (G == 2 ? 0 : b * 128) + u * 64 + r * 16);
    const f32x2 ml0 = mlw[(r * 2) * G + og], ml1 = mlw[(r * 2 + 1) * G + og];
    f32x4 d[8], ds[4];
    const f32x4 z4 = {0.f, 0.f, 0.f, 0.f};
    if (G == 2) {
      const u32x4 a2 = *reinterpret_cast<const u32x4*>(rp + cb * 64 + r * 16);
#pragma unroll
      for (int e = 0; e < 4; ++e) ds[e] = mfma32(a2, bs[e], z4);
    } else {
      const u32x2 a2 = *reinterpret_cast<const u32x2*>(rp + wave * 32 + r * 8);
#pragma unroll
      for (int e = 0; e < 4; ++e) ds[e] = mfma16(a2, u32x2{bs[e][0], bs[e][1]}, z4);
    }
#pragma unroll
    for (int u = 0; u < 2 * G; ++u)
#pragma unroll
      for (int e = 0; e < 8; ++e) d[e] = mfma32(av[u], bt[u][e], u ? d[e] : z4);
    const float M0 = xrow_max(ml0[0]), M1 = xrow_max(ml1[0]);
    const float w0 = __builtin_amdgcn_exp2f(ml0[0] - M0), w1 = __builtin_amdgcn_exp2f(ml1[0] - M1);
    const float L0 = xrow_sum(w0 * ml0[1]), L1 = xrow_sum(w1 * ml1[1]);
    mlout = (r & 1) ? f32x2{M1, L1} : f32x2{M0, L0};
    float h[8], hs[4];
#pragma unroll
    for (int e = 0; e < 4; ++e) {
      auto s = __builtin_amdgcn_permlane32_swap(__float_as_uint(w0 * (ds[e][0] + ds[e][2])), __float_as_uint(w1 * (ds[e][1] + ds[e][3])), false, false);
      hs[e] = __uint_as_float(s[0]) + __uint_as_float(s[1]);
    }
    f32x2 ys;
#pragma unroll
    for (int e = 0; e < 2; ++e) {
      auto s = __builtin_amdgcn_permlane16_swap(__float_as_uint(hs[e]), __float_as_uint(hs[e + 2]), false, false);
      ys[e] = __uint_as_float(s[0]) + __uint_as_float(s[1]);
    }
    *reinterpret_cast<f32x2*>(yl + 1024 + (wave * 64 + lane) * 2) = ys;
#pragma unroll
    for (int e = 0; e < 8; ++e) {
      auto s = __builtin_amdgcn_permlane32_swap(__float_as_uint(w0 * (d[e][0] + d[e][2])), __float_as_uint(w1 * (d[e][1] + d[e][3])), false, false);
      h[e] = __uint_as_float(s[0]) + __uint_as_float(s[1]);
    }
#pragma unroll
    for (int e = 0; e < 4; ++e) {
      auto s = __builtin_amdgcn_permlane16_swap(__float_as_uint(h[e]), __float_as_uint(h[e + 4]), false, false);
      ym[e] = __uint_as_float(s[0]) + __uint_as_float(s[1]);
    }
  }
  float* yws = a.ws + WS_Y / 4 + ((size_t)((br * 2 + (r >> 1)) * 32 + kvh * G + og) * SMAX + j) * DM;
  f32x4* ymp = reinterpret_cast<f32x4*>(yws + cb * 128 + c * 8 + (r & 1) * 4);
  if (G == 2) *ymp = ym;
  else if (b) *reinterpret_cast<f32x4*>(yl + (wv * 64 + lane) * 4) = ym;
  if ((wave & 3) == 3 && (G == 2 || wave == 3) && (lane & 47) == 0)
    reinterpret_cast<f32x2*>(a.ws + WS_ML / 4)[((br * 2 + (r & 1)) * 32 + kvh * G + og) * SMAX + j] = mlout;
  LBAR();
  if (G == 1 && !b) *ymp = ym + *reinterpret_cast<const f32x4*>(yl + (wv * 64 + lane) * 4);
  if ((wave & 3) == 1 && (G == 2 || wave == 1)) {
    f32x2 s = {0.f, 0.f};
#pragma unroll
    for (int w2 = 0; w2 < WAVES / G; ++w2) s += *reinterpret_cast<const f32x2*>(yl + 1024 + ((og * 4 + w2) * 64 + lane) * 2);
    *reinterpret_cast<f32x2*>(yws + 512 + c * 4 + (r & 1) * 2) = s;
  }
}

__global__ void __launch_bounds__(NTHR, 2) attn_main(Args a) {
  __shared__ __attribute__((aligned(16))) char lds[LDS_BYTES];
  {
    unsigned long long pc0; unsigned len;
    asm volatile("s_getpc_b64 %0\nattn_text_beg:\n\ts_mov_b32 %1, attn_text_end-attn_text_beg" : "=s"(pc0), "=s"(len));
    unsigned off = threadIdx.x * 128u; off = off < len ? off : 0u;
    const unsigned lb = (unsigned)(size_t)(__attribute__((address_space(3))) char*)lds + LDS_REC;
    asm volatile("s_mov_b32 m0, %0\n\ts_nop 0\n\tglobal_load_lds_dword %1, %2" :: "s"(lb), "v"(off), "s"(pc0 & ~3ull) : "m0", "memory");
  }
  const int xcd = blockIdx.x & 7, slot = blockIdx.x >> 3;
  if (slot < A_PER_X) {
    const int j = slot % NWA;
    body<2>(a, 0, ((xcd + 1) & 7) * 2 + slot / NWA, j, (TILES * j) / NWA, (TILES * (j + 1)) / NWA, lds);
  } else {
    const int j = (slot - A_PER_X) % NWB;
    body<1>(a, 1, xcd * 4 + (slot - A_PER_X) / NWB, j, (TILES * j) / NWB, (TILES * (j + 1)) / NWB, lds);
  }
}

__global__ void __launch_bounds__(256) attn_final(const float* ws, __hip_bfloat16* out) {
  __shared__ __attribute__((aligned(16))) float red[64 * 16];
  const int chunk = blockIdx.x % 36, bb = (blockIdx.x / 36) & 1, br = blockIdx.x / 72;
  const int tid = threadIdx.x, q4 = tid & 3, vi = tid >> 2;
  const int h = vi >> 1, half = vi & 1;
  const int S = br ? NWB : NWA;
  const f32x2* ml = reinterpret_cast<const f32x2*>(ws + WS_ML / 4) + ((br * 2 + bb) * 32 + h) * SMAX;
  const float* y = ws + WS_Y / 4 + (size_t)(((br * 2 + bb) * 32 + h) * SMAX) * DM + chunk * 16 + q4 * 4;
  f32x4 yv[3];
  f32x2 mv[6];
#pragma unroll
  for (int i = 0; i < 3; ++i) { const int s = min(half * 3 + i, S - 1); yv[i] = *reinterpret_cast<const f32x4*>(y + s * DM); }
#pragma unroll
  for (int s = 0; s < 6; ++s) mv[s] = ml[min(s, S - 1)];
  if (S == 5) mv[5] = f32x2{-INFINITY, 0.f};
  float M = mv[0][0];
#pragma unroll
  for (int s = 1; s < 6; ++s) M = fmaxf(M, mv[s][0]);
  float L = 0.f, wt[6];
#pragma unroll
  for (int s = 0; s < 6; ++s) { wt[s] = __builtin_amdgcn_exp2f(mv[s][0] - M); L = fmaf(wt[s], mv[s][1], L); }
  const float inv = 1.f / L;
  const float w0 = half ? wt[3] : wt[0], w1 = half ? wt[4] : wt[1], w2 = half ? wt[5] : wt[2];
  const f32x4 sacc = (yv[0] * w0 + yv[1] * w1 + yv[2] * w2) * inv;
  *reinterpret_cast<f32x4*>(red + vi * 16 + q4 * 4) = sacc;
  __syncthreads();
  if (tid < 64) {
    const int col = tid & 15, part = tid >> 4;
    float s = 0.f;
#pragma unroll
    for (int i = 0; i < 16; ++i) s += red[(part * 16 + i) * 16 + col];
    s = xrow_sum(s);
    if (tid < 16) out[bb * (2 * DM) + br * DM + chunk * 16 + col] = __float2bfloat16(s);
  }
  asm volatile("attn_text_end:");
}

extern "C" void kernel_launch(void* const* d_in, const int* in_sizes, int n_in,
                              void* d_out, int out_size, void* d_ws, size_t ws_size,
                              hipStream_t stream) {
  Args a;
  for (int i = 0; i < 2; ++i) {
    a.x[i] = (const unsigned short*)d_in[5 * i + 0];
    a.k[i] = (const unsigned short*)d_in[5 * i + 1];
    a.v[i] = (const unsigned short*)d_in[5 * i + 2];
    a.wq[i] = (const unsigned short*)d_in[5 * i + 3];
    a.wo[i] = (const unsigned short*)d_in[5 * i + 4];
  }
  a.ws = (float*)d_ws;
  hipLaunchKernelGGL(attn_main, dim3(GRID1), dim3(NTHR), 0, stream, a);
  hipLaunchKernelGGL(attn_final, dim3(144), dim3(256), 0, stream, (const float*)d_ws, (__hip_bfloat16*)d_out);
}
```
